```python
import torch

def _softmax(x, axis=-1):
    return torch.softmax(x, dim=axis)
def _gelu(x):
    return torch.nn.functional.gelu(x, approximate="tanh")
def _silu(x):
    return torch.nn.functional.silu(x)
def _sigmoid(x):
    return torch.sigmoid(x)
def _layernorm(x, axis=-1, eps=1e-5):
    m = x.mean(dim=axis, keepdim=True)
    v = x.var(dim=axis, keepdim=True, unbiased=False)
    return (x - m) / torch.sqrt(v + eps)
def _scatter_add(tgt, idx, src, axis=0):
    return tgt.clone().index_add_(axis, idx, src)
def _segment_sum(src, seg, n):
    out = torch.zeros((n,) + src.shape[1:], dtype=src.dtype)
    return out.index_add_(0, seg, src)
def _pad(x, pw):
    flat = tuple(v for pair in reversed(pw) for v in pair)
    return torch.nn.functional.pad(x, flat)

kernel_name = 'gated_mix__mha_block__glu_gated_geglu__prompt'

def setup_inputs():

    _w = (lambda a: (lambda t: t.to(torch.bfloat16) if t.is_floating_point() else t)(torch.as_tensor(a)))
    def _A_setup():
        B, S, D, H = 1, 2048, 512, 32
        return {
            "x": _w((lambda *s: torch.randn(*s, dtype=torch.bfloat16) * 0.05)(B, S, D)),
            "wqkv": _w((lambda *s: torch.randn(*s, dtype=torch.bfloat16) * 0.8)(D, 3 * D)),
            "wo": _w((lambda *s: torch.randn(*s, dtype=torch.bfloat16) * 0.05)(D, D)),
        }
    _A_in = _A_setup()

    def _B_setup():
        B, S, Dm, Df = 1, 2048, 512, 1376
        return {
            "x": _w((lambda *s: torch.randn(*s, dtype=torch.bfloat16) * 0.05)(B, S, Dm)),
            "w_gate": _w((lambda *s: torch.randn(*s, dtype=torch.bfloat16) * 3.2)(Dm, Df) * (Dm ** -0.5)),
            "w_up": _w((lambda *s: torch.randn(*s, dtype=torch.bfloat16) * 3.2)(Dm, Df) * (Dm ** -0.5)),
            "w_down": _w((lambda *s: torch.randn(*s, dtype=torch.bfloat16) * 3.2)(Df, Dm) * (Df ** -0.5)),
        }
    _B_in = _B_setup()

    return {
        "A_x": _A_in["x"],
        "A_wqkv": _A_in["wqkv"],
        "A_wo": _A_in["wo"],
        "B_x": _B_in["x"],
        "B_w_gate": _B_in["w_gate"],
        "B_w_up": _B_in["w_up"],
        "B_w_down": _B_in["w_down"],
        "gate_w": _w((lambda *s: torch.randn(*s, dtype=torch.bfloat16) * 0.05)(512) * 16.0),
    }

def reference(A_x, A_wqkv, A_wo, B_x, B_w_gate, B_w_up, B_w_down, gate_w):
    def _A_ref(x, wqkv, wo):
        B, S, D = x.shape
        H = 32
        Dh = D // H
        qkv = (lambda x, *d: x.permute(*d))((x @ wqkv).reshape(B, S, 3, H, Dh), 2, 0, 3, 1, 4)
        q, k, v = qkv[0], qkv[1], qkv[2]
        scores = torch.einsum("bhqd,bhkd->bhqk", q, k) * (Dh ** -0.5)
        p = _softmax(scores, axis=-1)
        o = torch.einsum("bhqk,bhkd->bhqd", p, v)
        return (lambda x, *d: x.permute(*d))(o, 0, 2, 1, 3).reshape(B, S, D) @ wo

    def _B_ref(x, w_gate, w_up, w_down):
        g = torch.einsum("bsd,df->bsf", x, w_gate)
        u = torch.einsum("bsd,df->bsf", x, w_up)
        h = _gelu(g) * u
        return torch.einsum("bsf,fd->bsd", h, w_down)

    x = A_x
    g = _sigmoid((x * gate_w).sum(-1, keepdims=True))
    a = _A_ref(x, A_wqkv, A_wo)
    b = _B_ref(x, B_w_gate, B_w_up, B_w_down)
    return g * a + (1 - g) * b
```

```cpp
#include <hip/hip_runtime.h>
#include <hip/hip_bf16.h>
#include <stdint.h>

#define ATT_PRO_ASM \
  "ds_read_b128 v[104:107], v129\n\t" \
  "ds_read_b128 v[108:111], v129 offset:2048\n\t" \
  "s_mov_b64 exec, %[msk]\n\t" \
  "ds_read_b128 v[112:115], v130\n\t" \
  "ds_read_b128 v[120:123], v130 offset:512\n\t" \
  "s_mov_b64 exec, -1\n\t" \
  "s_waitcnt lgkmcnt(0)\n\t" \
  "v_mfma_f32_32x32x16_bf16 v[32:47], v[104:107], v[96:99], v[64:79]\n\t" \
  "s_nop 15\n\t" \
  ""
#define ATT_LOOP_BODY \
  "1:\n\t" \
  "s_waitcnt lgkmcnt(0)\n\t" \
  "v_exp_f32 v32, v32\n\t" \
  "v_exp_f32 v33, v33\n\t" \
  "v_smfmac_f32_32x32x32_bf16 v[16:31], v[124:127], v[56:63], v128\n\t" \
  "v_exp_f32 v34, v34\n\t" \
  "v_exp_f32 v35, v35\n\t" \
  "v_mfma_f32_32x32x16_bf16 v[48:63], v[104:107], v[100:103], v[80:95]\n\t" \
  "v_exp_f32 v36, v36\n\t" \
  "v_exp_f32 v37, v37\n\t" \
  "v_exp_f32 v38, v38\n\t" \
  "v_exp_f32 v39, v39\n\t" \
  "v_exp_f32 v40, v40\n\t" \
  "v_exp_f32 v41, v41\n\t" \
  "v_smfmac_f32_32x32x32_bf16 v[0:15], v[112:115], v[32:39], v128\n\t" \
  "v_exp_f32 v42, v42\n\t" \
  "v_exp_f32 v43, v43\n\t" \
  "v_exp_f32 v44, v44\n\t" \
  "v_exp_f32 v45, v45\n\t" \
  "v_exp_f32 v46, v46\n\t" \
  "v_exp_f32 v47, v47\n\t" \
  "v_exp_f32 v48, v48\n\t" \
  "v_exp_f32 v49, v49\n\t" \
  "v_smfmac_f32_32x32x32_bf16 v[0:15], v[120:123], v[40:47], v128\n\t" \
  "s_mov_b64 exec, %[msk]\n\t" \
  "ds_read_b128 v[116:119], v130 offset:2048\n\t" \
  "ds_read_b128 v[124:127], v130 offset:2560\n\t" \
  "s_mov_b64 exec, -1\n\t" \
  "v_exp_f32 v50, v50\n\t" \
  "v_exp_f32 v51, v51\n\t" \
  "v_mfma_f32_32x32x16_bf16 v[32:47], v[108:111], v[96:99], v[64:79]\n\t" \
  "ds_read_b128 v[104:107], v129 offset:4096\n\t" \
  "v_exp_f32 v52, v52\n\t" \
  "v_exp_f32 v53, v53\n\t" \
  "v_exp_f32 v54, v54\n\t" \
  "v_exp_f32 v55, v55\n\t" \
  "v_exp_f32 v56, v56\n\t" \
  "v_exp_f32 v57, v57\n\t" \
  "v_smfmac_f32_32x32x32_bf16 v[16:31], v[112:115], v[48:55], v128\n\t" \
  "v_exp_f32 v58, v58\n\t" \
  "v_exp_f32 v59, v59\n\t" \
  "v_exp_f32 v60, v60\n\t" \
  "v_exp_f32 v61, v61\n\t" \
  "v_exp_f32 v62, v62\n\t" \
  "v_exp_f32 v63, v63\n\t" \
  "s_waitcnt lgkmcnt(0)\n\t" \
  "v_exp_f32 v32, v32\n\t" \
  "v_exp_f32 v33, v33\n\t" \
  "v_smfmac_f32_32x32x32_bf16 v[16:31], v[120:123], v[56:63], v128\n\t" \
  "v_exp_f32 v34, v34\n\t" \
  "v_exp_f32 v35, v35\n\t" \
  "v_mfma_f32_32x32x16_bf16 v[48:63], v[108:111], v[100:103], v[80:95]\n\t" \
  "v_exp_f32 v36, v36\n\t" \
  "v_exp_f32 v37, v37\n\t" \
  "v_exp_f32 v38, v38\n\t" \
  "v_exp_f32 v39, v39\n\t" \
  "v_exp_f32 v40, v40\n\t" \
  "v_exp_f32 v41, v41\n\t" \
  "v_smfmac_f32_32x32x32_bf16 v[0:15], v[116:119], v[32:39], v128\n\t" \
  "v_exp_f32 v42, v42\n\t" \
  "v_exp_f32 v43, v43\n\t" \
  "v_exp_f32 v44, v44\n\t" \
  "v_exp_f32 v45, v45\n\t" \
  "v_exp_f32 v46, v46\n\t" \
  "v_exp_f32 v47, v47\n\t" \
  "v_exp_f32 v48, v48\n\t" \
  "v_exp_f32 v49, v49\n\t" \
  "v_smfmac_f32_32x32x32_bf16 v[0:15], v[124:127], v[40:47], v128\n\t" \
  "s_mov_b64 exec, %[msk]\n\t" \
  "ds_read_b128 v[112:115], v130 offset:4096\n\t" \
  "ds_read_b128 v[120:123], v130 offset:4608\n\t" \
  "s_mov_b64 exec, -1\n\t" \
  "v_exp_f32 v50, v50\n\t" \
  "v_exp_f32 v51, v51\n\t" \
  "v_mfma_f32_32x32x16_bf16 v[32:47], v[104:107], v[96:99], v[64:79]\n\t" \
  "ds_read_b128 v[108:111], v129 offset:6144\n\t" \
  "v_exp_f32 v52, v52\n\t" \
  "v_exp_f32 v53, v53\n\t" \
  "v_exp_f32 v54, v54\n\t" \
  "v_exp_f32 v55, v55\n\t" \
  "v_exp_f32 v56, v56\n\t" \
  "v_exp_f32 v57, v57\n\t" \
  "v_smfmac_f32_32x32x32_bf16 v[16:31], v[116:119], v[48:55], v128\n\t" \
  "v_exp_f32 v58, v58\n\t" \
  "v_exp_f32 v59, v59\n\t" \
  "v_exp_f32 v60, v60\n\t" \
  "v_exp_f32 v61, v61\n\t" \
  "v_exp_f32 v62, v62\n\t" \
  "v_exp_f32 v63, v63\n\t" \
  "s_waitcnt lgkmcnt(0)\n\t" \
  "v_exp_f32 v32, v32\n\t" \
  "v_exp_f32 v33, v33\n\t" \
  "v_smfmac_f32_32x32x32_bf16 v[16:31], v[124:127], v[56:63], v128\n\t" \
  "v_exp_f32 v34, v34\n\t" \
  "v_exp_f32 v35, v35\n\t" \
  "v_mfma_f32_32x32x16_bf16 v[48:63], v[104:107], v[100:103], v[80:95]\n\t" \
  "v_exp_f32 v36, v36\n\t" \
  "v_exp_f32 v37, v37\n\t" \
  "v_exp_f32 v38, v38\n\t" \
  "v_exp_f32 v39, v39\n\t" \
  "v_exp_f32 v40, v40\n\t" \
  "v_exp_f32 v41, v41\n\t" \
  "v_smfmac_f32_32x32x32_bf16 v[0:15], v[112:115], v[32:39], v128\n\t" \
  "v_exp_f32 v42, v42\n\t" \
  "v_exp_f32 v43, v43\n\t" \
  "v_exp_f32 v44, v44\n\t" \
  "v_exp_f32 v45, v45\n\t" \
  "v_exp_f32 v46, v46\n\t" \
  "v_exp_f32 v47, v47\n\t" \
  "v_exp_f32 v48, v48\n\t" \
  "v_exp_f32 v49, v49\n\t" \
  "v_smfmac_f32_32x32x32_bf16 v[0:15], v[120:123], v[40:47], v128\n\t" \
  "s_mov_b64 exec, %[msk]\n\t" \
  "ds_read_b128 v[116:119], v130 offset:6144\n\t" \
  "ds_read_b128 v[124:127], v130 offset:6656\n\t" \
  "s_mov_b64 exec, -1\n\t" \
  "v_exp_f32 v50, v50\n\t" \
  "v_exp_f32 v51, v51\n\t" \
  "v_mfma_f32_32x32x16_bf16 v[32:47], v[108:111], v[96:99], v[64:79]\n\t" \
  "ds_read_b128 v[104:107], v129 offset:8192\n\t" \
  "v_exp_f32 v52, v52\n\t" \
  "v_exp_f32 v53, v53\n\t" \
  "v_exp_f32 v54, v54\n\t" \
  "v_exp_f32 v55, v55\n\t" \
  "v_exp_f32 v56, v56\n\t" \
  "v_exp_f32 v57, v57\n\t" \
  "v_smfmac_f32_32x32x32_bf16 v[16:31], v[112:115], v[48:55], v128\n\t" \
  "v_exp_f32 v58, v58\n\t" \
  "v_exp_f32 v59, v59\n\t" \
  "v_exp_f32 v60, v60\n\t" \
  "v_exp_f32 v61, v61\n\t" \
  "v_exp_f32 v62, v62\n\t" \
  "v_exp_f32 v63, v63\n\t" \
  "s_waitcnt lgkmcnt(0)\n\t" \
  "v_exp_f32 v32, v32\n\t" \
  "v_exp_f32 v33, v33\n\t" \
  "v_smfmac_f32_32x32x32_bf16 v[16:31], v[120:123], v[56:63], v128\n\t" \
  "v_exp_f32 v34, v34\n\t" \
  "v_exp_f32 v35, v35\n\t" \
  "v_mfma_f32_32x32x16_bf16 v[48:63], v[108:111], v[100:103], v[80:95]\n\t" \
  "v_exp_f32 v36, v36\n\t" \
  "v_exp_f32 v37, v37\n\t" \
  "v_exp_f32 v38, v38\n\t" \
  "v_exp_f32 v39, v39\n\t" \
  "v_exp_f32 v40, v40\n\t" \
  "v_exp_f32 v41, v41\n\t" \
  "v_smfmac_f32_32x32x32_bf16 v[0:15], v[116:119], v[32:39], v128\n\t" \
  "v_exp_f32 v42, v42\n\t" \
  "v_exp_f32 v43, v43\n\t" \
  "v_exp_f32 v44, v44\n\t" \
  "v_exp_f32 v45, v45\n\t" \
  "v_exp_f32 v46, v46\n\t" \
  "v_exp_f32 v47, v47\n\t" \
  "v_exp_f32 v48, v48\n\t" \
  "v_exp_f32 v49, v49\n\t" \
  "v_smfmac_f32_32x32x32_bf16 v[0:15], v[124:127], v[40:47], v128\n\t" \
  "s_mov_b64 exec, %[msk]\n\t" \
  "ds_read_b128 v[112:115], v130 offset:8192\n\t" \
  "ds_read_b128 v[120:123], v130 offset:8704\n\t" \
  "s_mov_b64 exec, -1\n\t" \
  "v_exp_f32 v50, v50\n\t" \
  "v_exp_f32 v51, v51\n\t" \
  "v_mfma_f32_32x32x16_bf16 v[32:47], v[104:107], v[96:99], v[64:79]\n\t" \
  "ds_read_b128 v[108:111], v129 offset:10240\n\t" \
  "v_exp_f32 v52, v52\n\t" \
  "v_exp_f32 v53, v53\n\t" \
  "v_exp_f32 v54, v54\n\t" \
  "v_exp_f32 v55, v55\n\t" \
  "v_exp_f32 v56, v56\n\t" \
  "v_exp_f32 v57, v57\n\t" \
  "v_smfmac_f32_32x32x32_bf16 v[16:31], v[116:119], v[48:55], v128\n\t" \
  "v_exp_f32 v58, v58\n\t" \
  "v_exp_f32 v59, v59\n\t" \
  "v_exp_f32 v60, v60\n\t" \
  "v_exp_f32 v61, v61\n\t" \
  "v_exp_f32 v62, v62\n\t" \
  "v_exp_f32 v63, v63\n\t" \
  "v_add_u32 v129, 8192, v129\n\t" \
  "v_add_u32 v130, 8192, v130\n\t" \
  "s_sub_u32 %[cnt], %[cnt], 1\n\t" \
  "s_cmp_lg_u32 %[cnt], 0\n\t" \
  "s_cbranch_scc1 1b\n\t" \
  ""
#define ATT_HLOAD_ASM \
  "global_load_dwordx4 v[132:135], %[hvo], s[40:41] sc1\n\t" \
  "s_add_u32 s40, s40, 0x10000\n\t" \
  "s_addc_u32 s41, s41, 0\n\t" \
  "global_load_dwordx4 v[136:139], %[hvo], s[40:41] sc1\n\t" \
  "s_add_u32 s40, s40, 0x10000\n\t" \
  "s_addc_u32 s41, s41, 0\n\t" \
  "global_load_dwordx4 v[140:143], %[hvo], s[40:41] sc1\n\t" \
  "s_add_u32 s40, s40, 0x10000\n\t" \
  "s_addc_u32 s41, s41, 0\n\t" \
  "global_load_dwordx4 v[144:147], %[hvo], s[40:41] sc1\n\t" \
  "s_add_u32 s40, s40, 0x10000\n\t" \
  "s_addc_u32 s41, s41, 0\n\t" \
  "global_load_dwordx4 v[148:151], %[hvo], s[40:41] sc1\n\t" \
  "s_add_u32 s40, s40, 0x10000\n\t" \
  "s_addc_u32 s41, s41, 0\n\t" \
  "global_load_dwordx4 v[152:155], %[hvo], s[40:41] sc1\n\t" \
  "s_add_u32 s40, s40, 0x10000\n\t" \
  "s_addc_u32 s41, s41, 0\n\t" \
  "global_load_dwordx4 v[156:159], %[hvo], s[40:41] sc1\n\t" \
  "s_add_u32 s40, s40, 0x10000\n\t" \
  "s_addc_u32 s41, s41, 0\n\t" \
  "global_load_dwordx4 v[160:163], %[hvo], s[40:41] sc1\n\t" \
  "s_add_u32 s40, s40, 0x10000\n\t" \
  "s_addc_u32 s41, s41, 0\n\t" \
  "global_load_dwordx4 v[164:167], %[hvo], s[40:41] sc1\n\t" \
  "s_add_u32 s40, s40, 0x10000\n\t" \
  "s_addc_u32 s41, s41, 0\n\t" \
  "global_load_dwordx4 v[168:171], %[hvo], s[40:41] sc1\n\t" \
  "s_add_u32 s40, s40, 0x10000\n\t" \
  "s_addc_u32 s41, s41, 0\n\t" \
  "global_load_dwordx4 v[172:175], %[hvo], s[40:41] sc1\n\t" \
  "s_add_u32 s40, s40, 0x10000\n\t" \
  "s_addc_u32 s41, s41, 0\n\t" \
  "global_load_dwordx4 v[176:179], %[hvo], s[40:41] sc1\n\t" \
  "s_add_u32 s40, s40, 0x10000\n\t" \
  "s_addc_u32 s41, s41, 0\n\t" \
  "global_load_dwordx4 v[180:183], %[hvo], s[40:41] sc1\n\t" \
  "s_add_u32 s40, s40, 0x10000\n\t" \
  "s_addc_u32 s41, s41, 0\n\t" \
  "global_load_dwordx4 v[184:187], %[hvo], s[40:41] sc1\n\t" \
  "s_add_u32 s40, s40, 0x10000\n\t" \
  "s_addc_u32 s41, s41, 0\n\t" \
  "global_load_dwordx4 v[188:191], %[hvo], s[40:41] sc1\n\t" \
  "s_add_u32 s40, s40, 0x10000\n\t" \
  "s_addc_u32 s41, s41, 0\n\t" \
  "global_load_dwordx4 v[192:195], %[hvo], s[40:41] sc1\n\t" \
  "s_add_u32 s40, s40, 0x10000\n\t" \
  "s_addc_u32 s41, s41, 0\n\t" \
  "global_load_dwordx4 v[196:199], %[hvo], s[40:41] sc1\n\t" \
  "s_add_u32 s40, s40, 0x10000\n\t" \
  "s_addc_u32 s41, s41, 0\n\t" \
  "global_load_dwordx4 v[200:203], %[hvo], s[40:41] sc1\n\t" \
  "s_add_u32 s40, s40, 0x10000\n\t" \
  "s_addc_u32 s41, s41, 0\n\t" \
  "global_load_dwordx4 v[204:207], %[hvo], s[40:41] sc1\n\t" \
  "s_add_u32 s40, s40, 0x10000\n\t" \
  "s_addc_u32 s41, s41, 0\n\t" \
  "global_load_dwordx4 v[208:211], %[hvo], s[40:41] sc1\n\t" \
  "s_add_u32 s40, s40, 0x10000\n\t" \
  "s_addc_u32 s41, s41, 0\n\t" \
  "global_load_dwordx4 v[212:215], %[hvo], s[40:41] sc1\n\t" \
  "s_add_u32 s40, s40, %[linc]\n\t" \
  "s_addc_u32 s41, s41, 0\n\t" \
  "global_load_dwordx4 v[216:219], %[hvo], s[40:41] sc1\n\t" \
  ""
#define ATT_EPI_ASM \
  "s_nop 1\n\t" \
  "v_smfmac_f32_32x32x32_bf16 v[16:31], v[124:127], v[56:63], v128\n\t" \
  "s_waitcnt lgkmcnt(0)\n\t" \
  "s_nop 15\n\t" \
  ""

namespace {

typedef float f32x2 __attribute__((ext_vector_type(2)));
typedef float f32x4 __attribute__((ext_vector_type(4)));
typedef float f32x8 __attribute__((ext_vector_type(8)));
typedef float f32x16 __attribute__((ext_vector_type(16)));
typedef unsigned u32x2 __attribute__((ext_vector_type(2)));
typedef unsigned u32x4 __attribute__((ext_vector_type(4)));
typedef unsigned u32x8 __attribute__((ext_vector_type(8)));
typedef unsigned u32x16 __attribute__((ext_vector_type(16)));
typedef __bf16 bf16x2 __attribute__((ext_vector_type(2)));
typedef __bf16 bf16x8 __attribute__((ext_vector_type(8)));
typedef __bf16 bf16x16 __attribute__((ext_vector_type(16)));
typedef short s16x4 __attribute__((ext_vector_type(4)));
typedef unsigned long long u64;
#define LDSP(T, a) ((__attribute__((address_space(3))) T*)(uintptr_t)(a))

constexpr size_t WS_E = 0;
constexpr size_t WS_F1 = 1024;
constexpr size_t WS_F2 = WS_F1 + 2048;
constexpr size_t WS_F3 = WS_F2 + 2048;
constexpr size_t WS_G = 8192;
constexpr size_t WS_Q = 65536;
constexpr size_t WS_KV = WS_Q + 32 * 65536;
constexpr size_t WS_H = WS_KV + 32 * 131072;
constexpr size_t WS_O = WS_H + 86 * 65536;
constexpr size_t WS_DBG = WS_O + 32 * 65536;
constexpr int LDS_BYTES = 163840;
constexpr int SLOT = 16384;
constexpr unsigned SPIN_CAP = 1u << 20;

#define MFMA16(a, b, c) __builtin_amdgcn_mfma_f32_16x16x32_bf16(__builtin_bit_cast(bf16x8, a), __builtin_bit_cast(bf16x8, b), c, 0, 0, 0)
#define MFMA32(a, b, c) __builtin_amdgcn_mfma_f32_32x32x16_bf16(__builtin_bit_cast(bf16x8, a), __builtin_bit_cast(bf16x8, b), c, 0, 0, 0)
#define WAITV_BAR(N) asm volatile("s_waitcnt vmcnt(" #N ") lgkmcnt(0)\n\ts_barrier" ::: "memory")
#define BAR() asm volatile("s_waitcnt lgkmcnt(0)\n\ts_barrier" ::: "memory")

__device__ __forceinline__ unsigned pk2(float lo, float hi) { f32x2 v = {lo, hi}; return __builtin_bit_cast(unsigned, __builtin_convertvector(v, bf16x2)); }
__device__ __forceinline__ float bflo(unsigned u) { return __uint_as_float(u << 16); }
__device__ __forceinline__ float bfhi(unsigned u) { return __uint_as_float(u & 0xffff0000u); }

__device__ __forceinline__ void glds2(unsigned voff0, unsigned voff1, const char* sbase, unsigned ldst) {
    unsigned sv;
    asm volatile("s_mov_b32 %0, m0\n\ts_mov_b32 m0, %4\n\ts_nop 0\n\tglobal_load_lds_dwordx4 %1, %3\n\t"
                 "s_mov_b32 m0, %5\n\ts_nop 0\n\tglobal_load_lds_dwordx4 %2, %3\n\ts_mov_b32 m0, %0"
                 : "=&s"(sv) : "v"(voff0), "v"(voff1), "s"(sbase), "s"(ldst), "s"(ldst + 1024u) : "memory");
}
__device__ __forceinline__ void glds1_sc1(unsigned voff, const char* sbase, unsigned ldst) {
    unsigned sv;
    asm volatile("s_mov_b32 %0, m0\n\ts_mov_b32 m0, %3\n\ts_nop 0\n\tglobal_load_lds_dwordx4 %1, %2 sc1\n\ts_mov_b32 m0, %0"
                 : "=&s"(sv) : "v"(voff), "s"(sbase), "s"(ldst) : "memory");
}
__device__ __forceinline__ void st16_sc1(const char* sbase, unsigned voff, u32x4 v) {
    asm volatile("global_store_dwordx4 %0, %1, %2 sc1\n\ts_nop 1" :: "v"(voff), "v"(v), "s"(sbase) : "memory");
}
__device__ __forceinline__ void st8_sc1(const char* sbase, unsigned voff, u64 v) {
    asm volatile("global_store_dwordx2 %0, %1, %2 sc1" :: "v"(voff), "v"(v), "s"(sbase) : "memory");
}
__device__ __forceinline__ void st4_sc1(const char* sbase, unsigned voff, unsigned v) {
    asm volatile("global_store_dword %0, %1, %2 sc1" :: "v"(voff), "v"(v), "s"(sbase) : "memory");
}
__device__ __forceinline__ u64 ld8_sc1_wait(const char* sbase, unsigned voff) {
    u64 v;
    asm volatile("global_load_dwordx2 %0, %1, %2 sc1\n\ts_waitcnt vmcnt(0)" : "=&v"(v) : "v"(voff), "s"(sbase) : "memory");
    return v;
}
__device__ __forceinline__ void poll_flags(const char* sbase, unsigned voff, u64 tag) {
    for (unsigned spins = 0; spins < SPIN_CAP; ++spins) {
        const u64 v = ld8_sc1_wait(sbase, voff);
        if (__all(v == tag)) break;
        __builtin_amdgcn_s_sleep(2);
    }
}
__device__ __forceinline__ u32x2 tr_read(unsigned a) {
    return __builtin_bit_cast(u32x2, __builtin_amdgcn_ds_read_tr16_b64_v4i16(LDSP(s16x4, a)));
}
__device__ __forceinline__ u32x4 tr_frag(unsigned a, unsigned second) {
    const u32x2 lo = tr_read(a), hi = tr_read(a + second);
    return (u32x4){lo[0], lo[1], hi[0], hi[1]};
}
__device__ __forceinline__ u32x4 pack_block(const f32x4& t0, const f32x4& t1) {
    unsigned a0 = pk2(t0[0], t0[1]), a1 = pk2(t0[2], t0[3]), b0 = pk2(t1[0], t1[1]), b1 = pk2(t1[2], t1[3]);
    auto r0 = __builtin_amdgcn_permlane16_swap(a0, b0, false, false);
    auto r1 = __builtin_amdgcn_permlane16_swap(a1, b1, false, false);
    return (u32x4){r0[0], r1[0], r0[1], r1[1]};
}
__device__ __forceinline__ float rowmax16(const f32x16& s) {
    float m = fmaxf(fmaxf(s[0], s[1]), s[2]);
#pragma unroll
    for (int r = 3; r < 15; r += 2) m = fmaxf(fmaxf(m, s[r]), s[r + 1]);
    m = fmaxf(m, s[15]);
    auto rr = __builtin_amdgcn_permlane32_swap(__float_as_uint(m), __float_as_uint(m), false, false);
    return fmaxf(__uint_as_float(rr[0]), __uint_as_float(rr[1]));
}
template <int CTRL> __device__ __forceinline__ float dpp_add(float v) {
    return v + __uint_as_float(__builtin_amdgcn_update_dpp(0u, __float_as_uint(v), CTRL, 0xf, 0xf, true));
}

#define STAMP(i) do { } while (0)

}

__global__ void __launch_bounds__(512, 2)
gated_mix_fused_kernel(const char* __restrict__ x, const char* __restrict__ wqkv, const char* __restrict__ wo, const char* __restrict__ wg,
                       const char* __restrict__ wu, const char* __restrict__ wd, const char* __restrict__ gate_w, char* __restrict__ out, char* ws) {
    constexpr unsigned L0 = 0;
    const int lane = threadIdx.x & 63;
    const int w = __builtin_amdgcn_readfirstlane(threadIdx.x >> 6);
    const int bid = blockIdx.x, xcd = bid & 7, jb = bid >> 3;
    STAMP(0);

    u64 epoch;
    asm volatile("global_load_dwordx2 %0, %1, %2 sc1" : "=v"(epoch) : "v"(0u), "s"(ws + WS_E) : "memory");

    const int tb = 8 * (xcd & 1) + (jb & 7), cg = 4 * (xcd >> 1) + (jb >> 3);
    const int tb32 = w & 3, hf = w >> 2;
    const int u0 = (cg * 80) / 15;
    unsigned vx[2], vq[2], vf[2];
#pragma unroll
    for (int par = 0; par < 2; ++par) {
        const int row = 16 * w + 8 * par + (lane >> 3);
        const int c16 = (lane & 7) ^ ((4 * par + (lane >> 4)) & 7);
        vx[par] = (unsigned)((128 * tb + row) * 1024 + c16 * 16);
        const int rr = lane >> 4, p = lane & 15, r = rr + 4 * (w & 1);
        const int t = (p - 4 * r) & 15;
        int u = (t >> 1) ^ (w & 1); if (u >= 6) u -= 2;
        const int cb = u * 32 + (t & 1) * 16;
        const int wrow = 8 * w + 4 * par + rr;
        vq[par] = (unsigned)(wrow * 3072 + 192 * cg + cb);
        vf[par] = (unsigned)((wrow & 31) * 2752 + 32 * u0 + cb);
    }
    const char* wf_base = (w < 4) ? wg : wu;
    const unsigned ldst_w = (unsigned)(2 * w) * 1024u;
#define SLOT_OF(n) (L0 + (unsigned)(((n) % 9) * SLOT))
#define FILL(n) do { if ((n) < 16) { if (((n) & 1) == 0) glds2(vx[0], vx[1], x + ((n) >> 1) * 128, SLOT_OF(n) + ldst_w); \
                                     else glds2(vq[0], vq[1], wqkv + (size_t)((n) >> 1) * 64 * 3072, SLOT_OF(n) + ldst_w); } \
                     else glds2(vf[0], vf[1], wf_base + (size_t)((n) - 16) * 32 * 2752, SLOT_OF(n) + ldst_w); } while (0)
#pragma unroll
    for (int n = 0; n < 9; ++n) FILL(n);

    const int kq4 = lane >> 4, l15 = lane & 15;
    unsigned wa[3];
    {
        const int q = l15 >> 2, pp = lane & 3, r = 4 * (kq4 & 1) + q, b2 = kq4 & 1;
#pragma unroll
        for (int i = 0; i < 3; ++i) { const int u = 3 * hf + i; wa[i] = (unsigned)((8 * kq4 + q) * 256 + ((((u ^ b2) + 2 * r) & 7) * 32) + 8 * pp); }
    }
    const unsigned xa = (unsigned)((32 * tb32 + l15) * 128 + ((kq4 ^ (l15 >> 1)) * 16));
    u32x4 xf[16][2];
    f32x4 aq[3][2];
#pragma unroll
    for (int i = 0; i < 3; ++i) { aq[i][0] = (f32x4){}; aq[i][1] = (f32x4){}; }

    u64 tag = 0;
#pragma unroll
    for (int c = 0; c < 8; ++c) {
        if (c == 0) { asm volatile("s_waitcnt vmcnt(14)\n\ts_barrier" : "+v"(epoch) :: "memory"); tag = epoch + 1; }
        else { WAITV_BAR(10); FILL(2 * c + 7); FILL(2 * c + 8); }
        unsigned x0 = xa + SLOT_OF(2 * c), x1 = (xa ^ 64u) + SLOT_OF(2 * c), w0 = wa[0] + SLOT_OF(2 * c + 1), w1 = wa[1] + SLOT_OF(2 * c + 1), w2 = wa[2] + SLOT_OF(2 * c + 1);
        asm volatile("" : "+v"(x0), "+v"(x1), "+v"(w0), "+v"(w1), "+v"(w2));
#pragma unroll
        for (int sub = 0; sub < 2; ++sub) {
            const int ks = 2 * c + sub;
#pragma unroll
            for (int tt = 0; tt < 2; ++tt) xf[ks][tt] = *LDSP(const u32x4, (sub ? x1 : x0) + tt * 2048);
#pragma unroll
            for (int i = 0; i < 3; ++i) {
                const u32x4 wf = tr_frag((i == 0 ? w0 : i == 1 ? w1 : w2) + sub * 8192, 1024);
                aq[i][0] = MFMA16(wf, xf[ks][0], aq[i][0]);
                aq[i][1] = MFMA16(wf, xf[ks][1], aq[i][1]);
            }
        }
    }
    STAMP(1);
    {
        const int tile32 = 4 * tb + tb32;
        const unsigned scr = L0 + 9 * SLOT + w * 1024;
#pragma unroll
        for (int i = 0; i < 3; ++i) {
            const int T = 6 * cg + 3 * hf + i, ty = T >> 5, head = T & 31;
            if (ty == 2) {
#pragma unroll
                for (int tt = 0; tt < 2; ++tt)
                    *LDSP(u32x2, scr + (16 * tt + l15) * 32 + kq4 * 8) = (u32x2){pk2(aq[i][tt][0], aq[i][tt][1]), pk2(aq[i][tt][2], aq[i][tt][3])};
                const u32x4 v = tr_frag(scr + (8 * kq4 + (l15 >> 2)) * 32 + 8 * (lane & 3), 128);
                st16_sc1(ws + WS_KV + (size_t)head * 131072 + (size_t)tile32 * 2048 + 1024, (unsigned)(lane * 16), v);
            } else {
                const float sc = (ty == 0) ? 0.25f * 1.4426950408889634f : 1.0f;
                const u32x4 v = pack_block(aq[i][0] * sc, aq[i][1] * sc);
                const unsigned off = (unsigned)((kq4 >> 1) * 512 + (16 * (kq4 & 1) + l15) * 16);
                const char* base = (ty == 0) ? ws + WS_Q + (size_t)head * 65536 + (size_t)tile32 * 1024
                                             : ws + WS_KV + (size_t)head * 131072 + (size_t)tile32 * 2048;
                st16_sc1(base, off, v);
            }
        }
    }
    f32x4 ag[3][2], au[3][2];
#pragma unroll
    for (int i = 0; i < 3; ++i) { ag[i][0] = ag[i][1] = au[i][0] = au[i][1] = (f32x4){}; }
#pragma unroll
    for (int s = 0; s < 16; ++s) {
        if (s == 0) { WAITV_BAR(15); FILL(23); FILL(24); }
        else if (s == 4) { WAITV_BAR(10); FILL(28);
                           if (threadIdx.x == 0) st8_sc1(ws + WS_F1 + (size_t)(cg * 16 + tb) * 8, 0u, tag); }
        else if (s <= 6) { WAITV_BAR(17); FILL(24 + s); }
        else if (s == 7) { WAITV_BAR(14); FILL(31); }
        else if (s == 8) { WAITV_BAR(14); }
        else if (s == 9) { WAITV_BAR(12); }
        else if (s == 10) { WAITV_BAR(10); }
        else if (s == 11) { WAITV_BAR(8); }
        else if (s == 12) { WAITV_BAR(6); }
        else if (s == 13) { WAITV_BAR(4); }
        else if (s == 14) { WAITV_BAR(2); }
        else { WAITV_BAR(0); }
        unsigned w0 = wa[0] + SLOT_OF(16 + s), w1 = wa[1] + SLOT_OF(16 + s), w2 = wa[2] + SLOT_OF(16 + s);
        asm volatile("" : "+v"(w0), "+v"(w1), "+v"(w2));
#pragma unroll
        for (int i = 0; i < 3; ++i) {
            const unsigned wb = i == 0 ? w0 : i == 1 ? w1 : w2;
            const u32x4 gf = tr_frag(wb, 1024), uf = tr_frag(wb + 8192, 1024);
            ag[i][0] = MFMA16(gf, xf[s][0], ag[i][0]);
            ag[i][1] = MFMA16(gf, xf[s][1], ag[i][1]);
            au[i][0] = MFMA16(uf, xf[s][0], au[i][0]);
            au[i][1] = MFMA16(uf, xf[s][1], au[i][1]);
        }
    }
    STAMP(2);
    const int ah = 4 * xcd + (jb >> 3), aqb = jb & 7;
    const int qg = w & 3, kh = w >> 2;
    if (w == 0) {
        const int cgq = ah / 6, cgk = (32 + ah) / 6, cgv = (64 + ah) / 6;
        const int li = lane < 34 ? lane : 33;
        const int fcg = li < 16 ? cgk : (li < 32 ? cgv : cgq), ftb = li < 32 ? (li & 15) : 2 * aqb + (li - 32);
        poll_flags(ws + WS_F1, (unsigned)((fcg * 16 + ftb) * 8), tag);
    }
    BAR();
    u32x4 qa, qbf, gx, gw;
    const int gtok = 128 * tb + 8 * cg + w;
    {
        asm volatile("global_load_dwordx4 %0, %2, %3\n\tglobal_load_dwordx4 %1, %2, %4" : "=&v"(gx), "=&v"(gw)
                     : "v"((unsigned)(lane * 16)), "s"(x + (size_t)gtok * 1024), "s"(gate_w) : "memory");
        const char* qp = ws + WS_Q + (size_t)ah * 65536 + (size_t)(8 * aqb + 2 * qg) * 1024;
        asm volatile("global_load_dwordx4 %0, %2, %3 sc1\n\tglobal_load_dwordx4 %1, %2, %3 offset:1024 sc1" : "=&v"(qa), "=&v"(qbf) : "v"((unsigned)(lane * 16)), "s"(qp) : "memory");
        const char* kvh = ws + WS_KV + (size_t)ah * 131072;
#pragma unroll
        for (int i = 0; i < 16; ++i) { const int piece = i * 8 + w; glds1_sc1((unsigned)(lane * 16), kvh + piece * 1024, L0 + piece * 1024); }
    }
    {
        const int tile32 = 4 * tb + tb32;
#pragma unroll
        for (int i = 0; i < 3; ++i) {
            f32x4 hv[2];
#pragma unroll
            for (int tt = 0; tt < 2; ++tt)
#pragma unroll
                for (int r = 0; r < 4; ++r) {
                    const float g = ag[i][tt][r];
                    const float e = __builtin_amdgcn_exp2f(g * __builtin_fmaf(g * g, -0.10294324f, -2.30220820f));
                    hv[tt][r] = g * au[i][tt][r] * __builtin_amdgcn_rcpf(1.0f + e);
                }
            const u32x4 v = pack_block(hv[0], hv[1]);
            const unsigned off = (unsigned)((kq4 >> 1) * 512 + (16 * (kq4 & 1) + l15) * 16);
            st16_sc1(ws + WS_H + (size_t)(u0 + 3 * hf + i) * 65536 + (size_t)tile32 * 1024, off, v);
            if (i == 0) {
                asm volatile("s_waitcnt vmcnt(19)" : "+v"(gx), "+v"(gw) :: "memory");
                float acc = 0.f;
#pragma unroll
                for (int e = 0; e < 4; ++e) { acc = __builtin_fmaf(bflo(gx[e]), bflo(gw[e]), acc); acc = __builtin_fmaf(bfhi(gx[e]), bfhi(gw[e]), acc); }
                acc = dpp_add<0x111>(acc); acc = dpp_add<0x112>(acc); acc = dpp_add<0x114>(acc); acc = dpp_add<0x118>(acc);
                const float tot = __uint_as_float(__builtin_amdgcn_readlane(__float_as_uint(acc), 15)) + __uint_as_float(__builtin_amdgcn_readlane(__float_as_uint(acc), 31))
                                + __uint_as_float(__builtin_amdgcn_readlane(__float_as_uint(acc), 47)) + __uint_as_float(__builtin_amdgcn_readlane(__float_as_uint(acc), 63));
                const float gate1 = __builtin_amdgcn_rcpf(1.0f + __builtin_amdgcn_exp2f(-1.4426950408889634f * tot));
                if (lane == 0) st4_sc1(ws + WS_G + (size_t)gtok * 4, 0u, __float_as_uint(gate1));
            }
        }
    }
    asm volatile("s_waitcnt vmcnt(4)\n\ts_barrier" : "+v"(qa), "+v"(qbf) :: "memory");
    STAMP(3);

    const int tb64 = 4 * xcd + (jb & 3), cb = jb >> 2;
    const int ptt = w & 1, pkq = w >> 1;
    const int fb0 = pkq * 22 - (pkq == 3 ? 1 : 0);
    const int nfb = pkq < 2 ? 22 : 21;

    const unsigned kaddr0 = L0 + lane * 16;
    const f32x16 z16 = {};
    f32x16 OA = {}, OB = {}, SA, SB = {};
    f32x16 NMA, NMB;
    {
        const u32x4 k0 = *LDSP(const u32x4, kaddr0);
        const float ma = rowmax16(MFMA32(k0, qa, z16)), mb = rowmax16(MFMA32(k0, qbf, z16));
#pragma unroll
        for (int r = 0; r < 16; ++r) { NMA[r] = -ma; NMB[r] = -mb; }
    }
    const unsigned one2 = 0x3F803F80u;
    const u32x4 ones = {one2, one2, one2, one2};
    u32x4 K0, K1, V00 = ones, V01 = ones, V10 = ones, V11 = ones;
    const unsigned idx = 0xDDDDDDDDu;
    unsigned kaddr = kaddr0 + kh * 65536, vaddr = L0 + 1024 + (lane >> 5) * 256 + l15 * 16 + kh * 65536;
    const u64 msk = 0x0000FFFF0000FFFFull;
    asm volatile(ATT_PRO_ASM
        : "={v[32:47]}"(SA), "={v[104:107]}"(K0), "={v[108:111]}"(K1), "+{v[112:115]}"(V00), "+{v[120:123]}"(V10)
        : "{v[64:79]}"(NMA), "{v[96:99]}"(qa), "{v129}"(kaddr), "{v130}"(vaddr), [msk] "s"(msk) : "memory");
    asm volatile("s_waitcnt vmcnt(0)\n\ts_barrier" ::: "memory");
    if (threadIdx.x == 0) st8_sc1(ws + WS_F2 + (size_t)(tb * 16 + cg) * 8, 0u, tag);
#define ATT_STATE "+{v[0:15]}"(OA), "+{v[16:31]}"(OB), "+{v[32:47]}"(SA), "+{v[48:63]}"(SB), "+{v[104:107]}"(K0), "+{v[108:111]}"(K1), \
                  "+{v[112:115]}"(V00), "+{v[116:119]}"(V01), "+{v[120:123]}"(V10), "+{v[124:127]}"(V11), "+{v129}"(kaddr), "+{v130}"(vaddr), [cnt] "+s"(cnt)
#define ATT_CONST "{v[64:79]}"(NMA), "{v[80:95]}"(NMB), "{v[96:99]}"(qa), "{v[100:103]}"(qbf), "{v128}"(idx), [msk] "s"(msk)
    int cnt = 2;
    asm volatile(ATT_LOOP_BODY : ATT_STATE : ATT_CONST : "memory", "scc");
    u64 f2v;
    const unsigned f2off = (unsigned)(((tb64 >> 1) * 16 + l15) * 8);
    cnt = 2;
    asm volatile("global_load_dwordx2 %[fv], %[fo], %[fb] sc1\n\t" ATT_LOOP_BODY "s_waitcnt vmcnt(0)"
        : ATT_STATE, [fv] "=&v"(f2v) : ATT_CONST, [fo] "v"(f2off), [fb] "s"(ws + WS_F2) : "memory", "scc");
    if (!__all(f2v == tag)) poll_flags(ws + WS_F2, f2off, tag);
    u32x16 H0, H1, H2, H3, H4; u32x8 H5; float gate;
    {
        const char* hp = ws + WS_H + (size_t)fb0 * 65536 + (size_t)(2 * tb64 + ptt) * 1024;
        const unsigned linc = pkq < 2 ? 0x10000u : 0u;
        cnt = 4;
        asm volatile("global_load_dword %[g], %[go], %[gb] sc1\n\t" ATT_HLOAD_ASM ATT_LOOP_BODY "s_waitcnt vmcnt(0)"
            : ATT_STATE, "=&{v[132:147]}"(H0), "=&{v[148:163]}"(H1), "=&{v[164:179]}"(H2), "=&{v[180:195]}"(H3), "=&{v[196:211]}"(H4), "=&{v[212:219]}"(H5),
              "+{s[40:41]}"(hp), [g] "=&v"(gate)
            : ATT_CONST, [hvo] "v"((unsigned)(lane * 16)), [linc] "s"(linc), [go] "v"((unsigned)((lane & 31) * 4)), [gb] "s"(ws + WS_G + (size_t)(64 * tb64 + 32 * ptt) * 4)
            : "memory", "scc");
    }
    asm volatile(ATT_EPI_ASM : "+{v[16:31]}"(OB) : "{v[48:63]}"(SB), "{v[124:127]}"(V11), "{v128}"(idx) : "memory");
    STAMP(4);
    {
        const unsigned xb = L0 + 131072 + w * 3072;
        const f32x16& snd = kh ? OA : OB;
        *LDSP(f32x4, xb + lane * 16) = (f32x4){snd[0], snd[1], snd[2], snd[3]};
        *LDSP(f32x4, xb + 1024 + lane * 16) = (f32x4){snd[4], snd[5], snd[6], snd[7]};
        *LDSP(float, xb + 2048 + lane * 4) = snd[8];
    }
    BAR();
    {
        float o[8], l;
        const unsigned pb = L0 + 131072 + (w ^ 4) * 3072;
        const f32x4 s0 = *LDSP(const f32x4, pb + lane * 16), s1 = *LDSP(const f32x4, pb + 1024 + lane * 16);
        const float sl = *LDSP(const float, pb + 2048 + lane * 4);
#pragma unroll
        for (int r = 0; r < 4; ++r) { o[r] = (kh ? OB[r] : OA[r]) + s0[r]; o[r + 4] = (kh ? OB[r + 4] : OA[r + 4]) + s1[r]; }
        l = (kh ? OB[8] : OA[8]) + sl;
        bool bad = !(l < 1.0e18f);
#pragma unroll
        for (int r = 0; r < 8; ++r) bad |= !(__builtin_fabsf(o[r]) < 3.0e38f);
        if (__builtin_expect(__any(bad), 0)) {
            const u32x4 qf = kh ? qbf : qa;
            float mr = -INFINITY;
            f32x16 acc = {};
#pragma unroll 1
            for (int t = 0; t < 64; ++t) {
                const u32x4 kf = *LDSP(const u32x4, kaddr0 + t * 2048);
                u32x4 v0 = ones, v1 = ones;
                if (!(lane & 16)) {
                    const unsigned va = L0 + 1024 + (lane >> 5) * 256 + l15 * 16 + t * 2048;
                    v0 = *LDSP(const u32x4, va); v1 = *LDSP(const u32x4, va + 512);
                }
                const f32x16 s = MFMA32(kf, qf, z16);
                const float mn = fmaxf(mr, rowmax16(s));
                const float al = __builtin_amdgcn_exp2f(mr - mn);
                mr = mn;
                f32x8 pa, pb2;
#pragma unroll
                for (int r = 0; r < 8; ++r) { pa[r] = __builtin_amdgcn_exp2f(s[r] - mn); pb2[r] = __builtin_amdgcn_exp2f(s[r + 8] - mn); }
                acc *= al;
                acc = __builtin_amdgcn_smfmac_f32_32x32x32_bf16(__builtin_bit_cast(bf16x8, v0), __builtin_bit_cast(bf16x16, pa), acc, (int)idx, 0, 0);
                acc = __builtin_amdgcn_smfmac_f32_32x32x32_bf16(__builtin_bit_cast(bf16x8, v1), __builtin_bit_cast(bf16x16, pb2), acc, (int)idx, 0, 0);
            }
#pragma unroll
            for (int r = 0; r < 8; ++r) o[r] = acc[r];
            l = acc[8];
        }
        const float rl = __builtin_amdgcn_rcpf(l);
        const unsigned a0 = pk2(o[0] * rl, o[1] * rl), a1 = pk2(o[2] * rl, o[3] * rl), b0 = pk2(o[4] * rl, o[5] * rl), b1 = pk2(o[6] * rl, o[7] * rl);
        auto r0 = __builtin_amdgcn_permlane32_swap(a0, b0, false, false);
        auto r1 = __builtin_amdgcn_permlane32_swap(a1, b1, false, false);
        st16_sc1(ws + WS_O + (size_t)ah * 65536 + (size_t)(8 * aqb + 2 * qg + kh) * 1024, (unsigned)(lane * 16), (u32x4){r0[0], r1[0], r0[1], r1[1]});
    }
    BAR();
    STAMP(5);
    unsigned vw[2];
#pragma unroll
    for (int par = 0; par < 2; ++par) { const int row = 8 * par + (lane >> 3); vw[par] = (unsigned)(row * 1024 + 128 * cb + 16 * ((lane & 7) ^ (4 * ((row >> 1) & 1)))); }
    const int fkq = w >> 1, fst = w & 1;
    const int ffb0 = fkq * 22 - (fkq == 3 ? 1 : 0), fnb = fkq < 2 ? 22 : 21;
#define WD_SLOT(n) (L0 + (unsigned)((4 + (n) % 5) * SLOT))
#define FILL_WD(n) do { const int s_ = 2 * (n) + fst; glds2(vw[0], vw[1], wd + (size_t)(ffb0 + (s_ < fnb ? s_ : fnb - 1)) * 16384, WD_SLOT(n) + ldst_w); } while (0)
#define FILL_WO(n) glds2(vw[0], vw[1], wo + (size_t)(8 * fkq + 2 * (n) + fst) * 16384, L0 + (unsigned)((n) * SLOT) + ldst_w)
#pragma unroll
    for (int n = 0; n < 5; ++n) FILL_WD(n);
#pragma unroll
    for (int n = 0; n < 4; ++n) FILL_WO(n);
    unsigned pa_[2];
    {
        const int g = lane >> 4, q = l15 >> 2, pp = lane & 3, row = 8 * (g >> 1) + q, c = 2 * (g & 1) + (pp >> 1);
        const unsigned a = (unsigned)(row * 128 + ((c ^ (4 * (q >> 1))) * 16) + 8 * (pp & 1));
        pa_[0] = a + pkq * 4096; pa_[1] = (a ^ 64u) + pkq * 4096;
    }
    f32x16 ab[2] = {z16, z16}, aa[2] = {z16, z16};
    const unsigned f3off = (unsigned)(((tb64 >> 2) * 32 + (lane & 31)) * 8);
    u64 f3v = 0;
#define HFRAG(s) ((s) < 4 ? __builtin_shufflevector(H0, H0, 4*((s)&3), 4*((s)&3)+1, 4*((s)&3)+2, 4*((s)&3)+3) : \
                  (s) < 8 ? __builtin_shufflevector(H1, H1, 4*((s)&3), 4*((s)&3)+1, 4*((s)&3)+2, 4*((s)&3)+3) : \
                  (s) < 12 ? __builtin_shufflevector(H2, H2, 4*((s)&3), 4*((s)&3)+1, 4*((s)&3)+2, 4*((s)&3)+3) : \
                  (s) < 16 ? __builtin_shufflevector(H3, H3, 4*((s)&3), 4*((s)&3)+1, 4*((s)&3)+2, 4*((s)&3)+3) : \
                  (s) < 20 ? __builtin_shufflevector(H4, H4, 4*((s)&3), 4*((s)&3)+1, 4*((s)&3)+2, 4*((s)&3)+3) : \
                             __builtin_shufflevector(H5, H5, 4*((s)&1), 4*((s)&1)+1, 4*((s)&1)+2, 4*((s)&1)+3))
#define P4_STEP(j, st) do { constexpr int s_ = 2 * (j) + (st); const u32x4 hf_ = HFRAG(s_); \
        if (s_ < 21 || pkq < 2) { \
            const u32x4 w0_ = tr_frag(b0_ + (st) * 2048, 512), w1_ = tr_frag(b1_ + (st) * 2048, 512); \
            ab[0] = MFMA32(w0_, hf_, ab[0]); ab[1] = MFMA32(w1_, hf_, ab[1]); } } while (0)
#define P4_STAGE(j) do { unsigned b0_ = pa_[0] + WD_SLOT(j), b1_ = pa_[1] + WD_SLOT(j); asm volatile("" : "+v"(b0_), "+v"(b1_)); P4_STEP(j, 0); P4_STEP(j, 1); } while (0)
    WAITV_BAR(16);
    if (threadIdx.x == 0) st8_sc1(ws + WS_F3 + (size_t)(aqb * 32 + ah) * 8, 0u, tag);
    P4_STAGE(0);
    WAITV_BAR(14); FILL_WD(5); P4_STAGE(1);
    WAITV_BAR(14); FILL_WD(6); P4_STAGE(2);
    WAITV_BAR(14); FILL_WD(7); P4_STAGE(3);
    WAITV_BAR(14); FILL_WD(8); P4_STAGE(4);
    WAITV_BAR(6); FILL_WD(9); P4_STAGE(5);
    WAITV_BAR(6); FILL_WD(10); P4_STAGE(6);
    WAITV_BAR(6); P4_STAGE(7);
    WAITV_BAR(4); P4_STAGE(8);
    WAITV_BAR(2);
    asm volatile("global_load_dwordx2 %0, %1, %2 sc1" : "=v"(f3v) : "v"(f3off), "s"(ws + WS_F3) : "memory");
    P4_STAGE(9);
    asm volatile("s_waitcnt vmcnt(0)\n\ts_barrier" : "+v"(f3v) :: "memory");
    if (!__all(f3v == tag)) poll_flags(ws + WS_F3, f3off, tag);
    u32x16 O0, O1;
    {
        const char* op = ws + WS_O + (size_t)(8 * pkq) * 65536 + (size_t)(2 * tb64 + ptt) * 1024;
        asm volatile("global_load_dwordx4 v[32:35], %[vo], s[40:41] sc1\n\ts_add_u32 s40, s40, 0x10000\n\ts_addc_u32 s41, s41, 0\n\t"
                     "global_load_dwordx4 v[36:39], %[vo], s[40:41] sc1\n\ts_add_u32 s40, s40, 0x10000\n\ts_addc_u32 s41, s41, 0\n\t"
                     "global_load_dwordx4 v[40:43], %[vo], s[40:41] sc1\n\ts_add_u32 s40, s40, 0x10000\n\ts_addc_u32 s41, s41, 0\n\t"
                     "global_load_dwordx4 v[44:47], %[vo], s[40:41] sc1\n\ts_add_u32 s40, s40, 0x10000\n\ts_addc_u32 s41, s41, 0\n\t"
                     "global_load_dwordx4 v[48:51], %[vo], s[40:41] sc1\n\ts_add_u32 s40, s40, 0x10000\n\ts_addc_u32 s41, s41, 0\n\t"
                     "global_load_dwordx4 v[52:55], %[vo], s[40:41] sc1\n\ts_add_u32 s40, s40, 0x10000\n\ts_addc_u32 s41, s41, 0\n\t"
                     "global_load_dwordx4 v[56:59], %[vo], s[40:41] sc1\n\ts_add_u32 s40, s40, 0x10000\n\ts_addc_u32 s41, s41, 0\n\t"
                     "global_load_dwordx4 v[60:63], %[vo], s[40:41] sc1"
                     : "=&{v[32:47]}"(O0), "=&{v[48:63]}"(O1), "+{s[40:41]}"(op) : [vo] "v"((unsigned)(lane * 16)) : "memory", "scc");
    }
    P4_STAGE(10);
    STAMP(6);
    asm volatile("s_waitcnt vmcnt(0)" : "+{v[32:47]}"(O0), "+{v[48:63]}"(O1) :: "memory");
#pragma unroll
    for (int s = 0; s < 8; ++s) {
        const u32x4 of = s < 4 ? (u32x4){O0[4 * (s & 3)], O0[4 * (s & 3) + 1], O0[4 * (s & 3) + 2], O0[4 * (s & 3) + 3]}
                               : (u32x4){O1[4 * (s & 3)], O1[4 * (s & 3) + 1], O1[4 * (s & 3) + 2], O1[4 * (s & 3) + 3]};
        const unsigned sl = L0 + (unsigned)((s >> 1) * SLOT + (s & 1) * 2048);
        const u32x4 w0 = tr_frag(sl + pa_[0], 512), w1 = tr_frag(sl + pa_[1], 512);
        aa[0] = MFMA32(w0, of, aa[0]); aa[1] = MFMA32(w1, of, aa[1]);
    }
    BAR();
    {
        const int tok = lane & 31, hh = lane >> 5;
        const float g1 = 1.0f - gate;
#pragma unroll
        for (int nt = 0; nt < 2; ++nt)
#pragma unroll
            for (int qd = 0; qd < 4; ++qd) {
                f32x4 v;
#pragma unroll
                for (int r = 0; r < 4; ++r) v[r] = gate * aa[nt][4 * qd + r] + g1 * ab[nt][4 * qd + r];
                const int ch = 8 * nt + 2 * qd + hh;
                *LDSP(f32x4, L0 + w * 8192 + tok * 256 + ((ch ^ (tok & 7)) * 16)) = v;
            }
    }
    BAR();
    {
        const int tok = 8 * pkq + (lane >> 3), c8 = lane & 7;
        f32x4 s0 = {}, s1 = {};
#pragma unroll
        for (int k = 0; k < 4; ++k) {
            const unsigned b = L0 + (unsigned)((2 * k + ptt) * 8192 + tok * 256);
            s0 += *LDSP(const f32x4, b + (((2 * c8) ^ (tok & 7)) * 16));
            s1 += *LDSP(const f32x4, b + (((2 * c8 + 1) ^ (tok & 7)) * 16));
        }
        const u32x4 v = {pk2(s0[0], s0[1]), pk2(s0[2], s0[3]), pk2(s1[0], s1[1]), pk2(s1[2], s1[3])};
        *(u32x4*)(out + (size_t)(64 * tb64 + 32 * ptt + tok) * 1024 + 128 * cb + 16 * c8) = v;
    }
    STAMP(7);
    if (bid == 0 && w == 1) {
#pragma unroll 1
        for (int k = 0; k < 4; ++k) poll_flags(ws + WS_F1, (unsigned)((k * 64 + lane) * 8), tag);
        if (lane == 0) st8_sc1(ws + WS_E, 0u, tag);
    }
}

extern "C" void kernel_launch(void* const* d_in, const int* in_sizes, int n_in, void* d_out, int out_size, void* d_ws, size_t ws_size,
                              hipStream_t stream) {
    (void)in_sizes; (void)n_in; (void)out_size; (void)ws_size;
    (void)hipFuncSetAttribute((const void*)gated_mix_fused_kernel, hipFuncAttributeMaxDynamicSharedMemorySize, LDS_BYTES);
    gated_mix_fused_kernel<<<256, 512, LDS_BYTES, stream>>>((const char*)d_in[0], (const char*)d_in[1], (const char*)d_in[2], (const char*)d_in[4],
                                                            (const char*)d_in[5], (const char*)d_in[6], (const char*)d_in[7], (char*)d_out, (char*)d_ws);
}
```

```cpp
#include <hip/hip_runtime.h>
#include <hip/hip_bf16.h>
#include <stdint.h>

#define ATT_PRO_ASM \
  "ds_read_b128 v[104:107], v129\n\t" \
  "ds_read_b128 v[108:111], v129 offset:2048\n\t" \
  "s_mov_b64 exec, %[msk]\n\t" \
  "ds_read_b128 v[112:115], v130\n\t" \
  "ds_read_b128 v[120:123], v130 offset:512\n\t" \
  "s_mov_b64 exec, -1\n\t" \
  "s_waitcnt lgkmcnt(0)\n\t" \
  "v_mfma_f32_32x32x16_bf16 v[32:47], v[104:107], v[96:99], v[64:79]\n\t" \
  "s_nop 15\n\t" \
  ""
#define ATT_LOOP_BODY \
  "1:\n\t" \
  "s_waitcnt lgkmcnt(0)\n\t" \
  "v_exp_f32 v32, v32\n\t" \
  "v_exp_f32 v33, v33\n\t" \
  "v_smfmac_f32_32x32x32_bf16 v[16:31], v[124:127], v[56:63], v128\n\t" \
  "v_exp_f32 v34, v34\n\t" \
  "v_exp_f32 v35, v35\n\t" \
  "v_mfma_f32_32x32x16_bf16 v[48:63], v[104:107], v[100:103], v[80:95]\n\t" \
  "v_exp_f32 v36, v36\n\t" \
  "v_exp_f32 v37, v37\n\t" \
  "v_exp_f32 v38, v38\n\t" \
  "v_exp_f32 v39, v39\n\t" \
  "v_exp_f32 v40, v40\n\t" \
  "v_exp_f32 v41, v41\n\t" \
  "v_smfmac_f32_32x32x32_bf16 v[0:15], v[112:115], v[32:39], v128\n\t" \
  "v_exp_f32 v42, v42\n\t" \
  "v_exp_f32 v43, v43\n\t" \
  "v_exp_f32 v44, v44\n\t" \
  "v_exp_f32 v45, v45\n\t" \
  "v_exp_f32 v46, v46\n\t" \
  "v_exp_f32 v47, v47\n\t" \
  "v_exp_f32 v48, v48\n\t" \
  "v_exp_f32 v49, v49\n\t" \
  "v_smfmac_f32_32x32x32_bf16 v[0:15], v[120:123], v[40:47], v128\n\t" \
  "s_mov_b64 exec, %[msk]\n\t" \
  "ds_read_b128 v[116:119], v130 offset:2048\n\t" \
  "ds_read_b128 v[124:127], v130 offset:2560\n\t" \
  "s_mov_b64 exec, -1\n\t" \
  "v_exp_f32 v50, v50\n\t" \
  "v_exp_f32 v51, v51\n\t" \
  "v_mfma_f32_32x32x16_bf16 v[32:47], v[108:111], v[96:99], v[64:79]\n\t" \
  "ds_read_b128 v[104:107], v129 offset:4096\n\t" \
  "v_exp_f32 v52, v52\n\t" \
  "v_exp_f32 v53, v53\n\t" \
  "v_exp_f32 v54, v54\n\t" \
  "v_exp_f32 v55, v55\n\t" \
  "v_exp_f32 v56, v56\n\t" \
  "v_exp_f32 v57, v57\n\t" \
  "v_smfmac_f32_32x32x32_bf16 v[16:31], v[112:115], v[48:55], v128\n\t" \
  "v_exp_f32 v58, v58\n\t" \
  "v_exp_f32 v59, v59\n\t" \
  "v_exp_f32 v60, v60\n\t" \
  "v_exp_f32 v61, v61\n\t" \
  "v_exp_f32 v62, v62\n\t" \
  "v_exp_f32 v63, v63\n\t" \
  "s_waitcnt lgkmcnt(0)\n\t" \
  "v_exp_f32 v32, v32\n\t" \
  "v_exp_f32 v33, v33\n\t" \
  "v_smfmac_f32_32x32x32_bf16 v[16:31], v[120:123], v[56:63], v128\n\t" \
  "v_exp_f32 v34, v34\n\t" \
  "v_exp_f32 v35, v35\n\t" \
  "v_mfma_f32_32x32x16_bf16 v[48:63], v[108:111], v[100:103], v[80:95]\n\t" \
  "v_exp_f32 v36, v36\n\t" \
  "v_exp_f32 v37, v37\n\t" \
  "v_exp_f32 v38, v38\n\t" \
  "v_exp_f32 v39, v39\n\t" \
  "v_exp_f32 v40, v40\n\t" \
  "v_exp_f32 v41, v41\n\t" \
  "v_smfmac_f32_32x32x32_bf16 v[0:15], v[116:119], v[32:39], v128\n\t" \
  "v_exp_f32 v42, v42\n\t" \
  "v_exp_f32 v43, v43\n\t" \
  "v_exp_f32 v44, v44\n\t" \
  "v_exp_f32 v45, v45\n\t" \
  "v_exp_f32 v46, v46\n\t" \
  "v_exp_f32 v47, v47\n\t" \
  "v_exp_f32 v48, v48\n\t" \
  "v_exp_f32 v49, v49\n\t" \
  "v_smfmac_f32_32x32x32_bf16 v[0:15], v[124:127], v[40:47], v128\n\t" \
  "s_mov_b64 exec, %[msk]\n\t" \
  "ds_read_b128 v[112:115], v130 offset:4096\n\t" \
  "ds_read_b128 v[120:123], v130 offset:4608\n\t" \
  "s_mov_b64 exec, -1\n\t" \
  "v_exp_f32 v50, v50\n\t" \
  "v_exp_f32 v51, v51\n\t" \
  "v_mfma_f32_32x32x16_bf16 v[32:47], v[104:107], v[96:99], v[64:79]\n\t" \
  "ds_read_b128 v[108:111], v129 offset:6144\n\t" \
  "v_exp_f32 v52, v52\n\t" \
  "v_exp_f32 v53, v53\n\t" \
  "v_exp_f32 v54, v54\n\t" \
  "v_exp_f32 v55, v55\n\t" \
  "v_exp_f32 v56, v56\n\t" \
  "v_exp_f32 v57, v57\n\t" \
  "v_smfmac_f32_32x32x32_bf16 v[16:31], v[116:119], v[48:55], v128\n\t" \
  "v_exp_f32 v58, v58\n\t" \
  "v_exp_f32 v59, v59\n\t" \
  "v_exp_f32 v60, v60\n\t" \
  "v_exp_f32 v61, v61\n\t" \
  "v_exp_f32 v62, v62\n\t" \
  "v_exp_f32 v63, v63\n\t" \
  "s_waitcnt lgkmcnt(0)\n\t" \
  "v_exp_f32 v32, v32\n\t" \
  "v_exp_f32 v33, v33\n\t" \
  "v_smfmac_f32_32x32x32_bf16 v[16:31], v[124:127], v[56:63], v128\n\t" \
  "v_exp_f32 v34, v34\n\t" \
  "v_exp_f32 v35, v35\n\t" \
  "v_mfma_f32_32x32x16_bf16 v[48:63], v[104:107], v[100:103], v[80:95]\n\t" \
  "v_exp_f32 v36, v36\n\t" \
  "v_exp_f32 v37, v37\n\t" \
  "v_exp_f32 v38, v38\n\t" \
  "v_exp_f32 v39, v39\n\t" \
  "v_exp_f32 v40, v40\n\t" \
  "v_exp_f32 v41, v41\n\t" \
  "v_smfmac_f32_32x32x32_bf16 v[0:15], v[112:115], v[32:39], v128\n\t" \
  "v_exp_f32 v42, v42\n\t" \
  "v_exp_f32 v43, v43\n\t" \
  "v_exp_f32 v44, v44\n\t" \
  "v_exp_f32 v45, v45\n\t" \
  "v_exp_f32 v46, v46\n\t" \
  "v_exp_f32 v47, v47\n\t" \
  "v_exp_f32 v48, v48\n\t" \
  "v_exp_f32 v49, v49\n\t" \
  "v_smfmac_f32_32x32x32_bf16 v[0:15], v[120:123], v[40:47], v128\n\t" \
  "s_mov_b64 exec, %[msk]\n\t" \
  "ds_read_b128 v[116:119], v130 offset:6144\n\t" \
  "ds_read_b128 v[124:127], v130 offset:6656\n\t" \
  "s_mov_b64 exec, -1\n\t" \
  "v_exp_f32 v50, v50\n\t" \
  "v_exp_f32 v51, v51\n\t" \
  "v_mfma_f32_32x32x16_bf16 v[32:47], v[108:111], v[96:99], v[64:79]\n\t" \
  "ds_read_b128 v[104:107], v129 offset:8192\n\t" \
  "v_exp_f32 v52, v52\n\t" \
  "v_exp_f32 v53, v53\n\t" \
  "v_exp_f32 v54, v54\n\t" \
  "v_exp_f32 v55, v55\n\t" \
  "v_exp_f32 v56, v56\n\t" \
  "v_exp_f32 v57, v57\n\t" \
  "v_smfmac_f32_32x32x32_bf16 v[16:31], v[112:115], v[48:55], v128\n\t" \
  "v_exp_f32 v58, v58\n\t" \
  "v_exp_f32 v59, v59\n\t" \
  "v_exp_f32 v60, v60\n\t" \
  "v_exp_f32 v61, v61\n\t" \
  "v_exp_f32 v62, v62\n\t" \
  "v_exp_f32 v63, v63\n\t" \
  "s_waitcnt lgkmcnt(0)\n\t" \
  "v_exp_f32 v32, v32\n\t" \
  "v_exp_f32 v33, v33\n\t" \
  "v_smfmac_f32_32x32x32_bf16 v[16:31], v[120:123], v[56:63], v128\n\t" \
  "v_exp_f32 v34, v34\n\t" \
  "v_exp_f32 v35, v35\n\t" \
  "v_mfma_f32_32x32x16_bf16 v[48:63], v[108:111], v[100:103], v[80:95]\n\t" \
  "v_exp_f32 v36, v36\n\t" \
  "v_exp_f32 v37, v37\n\t" \
  "v_exp_f32 v38, v38\n\t" \
  "v_exp_f32 v39, v39\n\t" \
  "v_exp_f32 v40, v40\n\t" \
  "v_exp_f32 v41, v41\n\t" \
  "v_smfmac_f32_32x32x32_bf16 v[0:15], v[116:119], v[32:39], v128\n\t" \
  "v_exp_f32 v42, v42\n\t" \
  "v_exp_f32 v43, v43\n\t" \
  "v_exp_f32 v44, v44\n\t" \
  "v_exp_f32 v45, v45\n\t" \
  "v_exp_f32 v46, v46\n\t" \
  "v_exp_f32 v47, v47\n\t" \
  "v_exp_f32 v48, v48\n\t" \
  "v_exp_f32 v49, v49\n\t" \
  "v_smfmac_f32_32x32x32_bf16 v[0:15], v[124:127], v[40:47], v128\n\t" \
  "s_mov_b64 exec, %[msk]\n\t" \
  "ds_read_b128 v[112:115], v130 offset:8192\n\t" \
  "ds_read_b128 v[120:123], v130 offset:8704\n\t" \
  "s_mov_b64 exec, -1\n\t" \
  "v_exp_f32 v50, v50\n\t" \
  "v_exp_f32 v51, v51\n\t" \
  "v_mfma_f32_32x32x16_bf16 v[32:47], v[104:107], v[96:99], v[64:79]\n\t" \
  "ds_read_b128 v[108:111], v129 offset:10240\n\t" \
  "v_exp_f32 v52, v52\n\t" \
  "v_exp_f32 v53, v53\n\t" \
  "v_exp_f32 v54, v54\n\t" \
  "v_exp_f32 v55, v55\n\t" \
  "v_exp_f32 v56, v56\n\t" \
  "v_exp_f32 v57, v57\n\t" \
  "v_smfmac_f32_32x32x32_bf16 v[16:31], v[116:119], v[48:55], v128\n\t" \
  "v_exp_f32 v58, v58\n\t" \
  "v_exp_f32 v59, v59\n\t" \
  "v_exp_f32 v60, v60\n\t" \
  "v_exp_f32 v61, v61\n\t" \
  "v_exp_f32 v62, v62\n\t" \
  "v_exp_f32 v63, v63\n\t" \
  "v_add_u32 v129, 8192, v129\n\t" \
  "v_add_u32 v130, 8192, v130\n\t" \
  "s_sub_u32 %[cnt], %[cnt], 1\n\t" \
  "s_cmp_lg_u32 %[cnt], 0\n\t" \
  "s_cbranch_scc1 1b\n\t" \
  ""
#define ATT_HLOAD_ASM \
  "global_load_dwordx4 v[132:135], %[hvo], s[40:41] sc1\n\t" \
  "s_add_u32 s40, s40, 0x10000\n\t" \
  "s_addc_u32 s41, s41, 0\n\t" \
  "global_load_dwordx4 v[136:139], %[hvo], s[40:41] sc1\n\t" \
  "s_add_u32 s40, s40, 0x10000\n\t" \
  "s_addc_u32 s41, s41, 0\n\t" \
  "global_load_dwordx4 v[140:143], %[hvo], s[40:41] sc1\n\t" \
  "s_add_u32 s40, s40, 0x10000\n\t" \
  "s_addc_u32 s41, s41, 0\n\t" \
  "global_load_dwordx4 v[144:147], %[hvo], s[40:41] sc1\n\t" \
  "s_add_u32 s40, s40, 0x10000\n\t" \
  "s_addc_u32 s41, s41, 0\n\t" \
  "global_load_dwordx4 v[148:151], %[hvo], s[40:41] sc1\n\t" \
  "s_add_u32 s40, s40, 0x10000\n\t" \
  "s_addc_u32 s41, s41, 0\n\t" \
  "global_load_dwordx4 v[152:155], %[hvo], s[40:41] sc1\n\t" \
  "s_add_u32 s40, s40, 0x10000\n\t" \
  "s_addc_u32 s41, s41, 0\n\t" \
  "global_load_dwordx4 v[156:159], %[hvo], s[40:41] sc1\n\t" \
  "s_add_u32 s40, s40, 0x10000\n\t" \
  "s_addc_u32 s41, s41, 0\n\t" \
  "global_load_dwordx4 v[160:163], %[hvo], s[40:41] sc1\n\t" \
  "s_add_u32 s40, s40, 0x10000\n\t" \
  "s_addc_u32 s41, s41, 0\n\t" \
  "global_load_dwordx4 v[164:167], %[hvo], s[40:41] sc1\n\t" \
  "s_add_u32 s40, s40, 0x10000\n\t" \
  "s_addc_u32 s41, s41, 0\n\t" \
  "global_load_dwordx4 v[168:171], %[hvo], s[40:41] sc1\n\t" \
  "s_add_u32 s40, s40, 0x10000\n\t" \
  "s_addc_u32 s41, s41, 0\n\t" \
  "global_load_dwordx4 v[172:175], %[hvo], s[40:41] sc1\n\t" \
  "s_add_u32 s40, s40, 0x10000\n\t" \
  "s_addc_u32 s41, s41, 0\n\t" \
  "global_load_dwordx4 v[176:179], %[hvo], s[40:41] sc1\n\t" \
  "s_add_u32 s40, s40, 0x10000\n\t" \
  "s_addc_u32 s41, s41, 0\n\t" \
  "global_load_dwordx4 v[180:183], %[hvo], s[40:41] sc1\n\t" \
  "s_add_u32 s40, s40, 0x10000\n\t" \
  "s_addc_u32 s41, s41, 0\n\t" \
  "global_load_dwordx4 v[184:187], %[hvo], s[40:41] sc1\n\t" \
  "s_add_u32 s40, s40, 0x10000\n\t" \
  "s_addc_u32 s41, s41, 0\n\t" \
  "global_load_dwordx4 v[188:191], %[hvo], s[40:41] sc1\n\t" \
  "s_add_u32 s40, s40, 0x10000\n\t" \
  "s_addc_u32 s41, s41, 0\n\t" \
  "global_load_dwordx4 v[192:195], %[hvo], s[40:41] sc1\n\t" \
  "s_add_u32 s40, s40, 0x10000\n\t" \
  "s_addc_u32 s41, s41, 0\n\t" \
  "global_load_dwordx4 v[196:199], %[hvo], s[40:41] sc1\n\t" \
  "s_add_u32 s40, s40, 0x10000\n\t" \
  "s_addc_u32 s41, s41, 0\n\t" \
  "global_load_dwordx4 v[200:203], %[hvo], s[40:41] sc1\n\t" \
  "s_add_u32 s40, s40, 0x10000\n\t" \
  "s_addc_u32 s41, s41, 0\n\t" \
  "global_load_dwordx4 v[204:207], %[hvo], s[40:41] sc1\n\t" \
  "s_add_u32 s40, s40, 0x10000\n\t" \
  "s_addc_u32 s41, s41, 0\n\t" \
  "global_load_dwordx4 v[208:211], %[hvo], s[40:41] sc1\n\t" \
  "s_add_u32 s40, s40, 0x10000\n\t" \
  "s_addc_u32 s41, s41, 0\n\t" \
  "global_load_dwordx4 v[212:215], %[hvo], s[40:41] sc1\n\t" \
  "s_add_u32 s40, s40, %[linc]\n\t" \
  "s_addc_u32 s41, s41, 0\n\t" \
  "global_load_dwordx4 v[216:219], %[hvo], s[40:41] sc1\n\t" \
  ""
#define ATT_EPI_ASM \
  "s_nop 1\n\t" \
  "v_smfmac_f32_32x32x32_bf16 v[16:31], v[124:127], v[56:63], v128\n\t" \
  "s_waitcnt lgkmcnt(0)\n\t" \
  "s_nop 15\n\t" \
  ""
#define P1_ASM_2 \
  "s_mov_b32 %[m0s], m0\n\t" \
  "v_mov_b32 v83, 0\n\t" \
  "global_load_dwordx2 %[ep], v83, %[epp] sc1\n\t" \
  "s_mov_b32 s52, %[fi]\n\t" \
  "s_mov_b32 s53, %[fi]\n\t" \
  "v_xor_b32 v80, 32, %[xa]\n\t" \
  "v_xor_b32 v81, 64, %[xa]\n\t" \
  "v_xor_b32 v82, 0x60, %[xa]\n\t" \
  "s_mul_i32 s44, s52, 128\n\t" \
  "s_add_u32 s44, s40, s44\n\t" \
  "s_addc_u32 s45, s41, 0\n\t" \
  "s_add_u32 m0, %[sx], 0\n\t" \
  "s_nop 0\n\t" \
  "global_load_lds_dwordx4 %[vx0], s[44:45]\n\t" \
  "s_add_u32 m0, %[sx], 1024\n\t" \
  "s_nop 0\n\t" \
  "global_load_lds_dwordx4 %[vx1], s[44:45]\n\t" \
  "s_add_u32 s52, s52, 1\n\t" \
  "s_and_b32 s52, s52, 7\n\t" \
  "s_mul_i32 s44, s53, 196608\n\t" \
  "s_add_u32 s44, s42, s44\n\t" \
  "s_addc_u32 s45, s43, 0\n\t" \
  "s_add_u32 m0, %[sq], 65536\n\t" \
  "s_nop 0\n\t" \
  "global_load_lds_dwordx4 %[vq0], s[44:45]\n\t" \
  "s_add_u32 s53, s53, 1\n\t" \
  "s_and_b32 s53, s53, 7\n\t" \
  "s_mul_i32 s44, s52, 128\n\t" \
  "s_add_u32 s44, s40, s44\n\t" \
  "s_addc_u32 s45, s41, 0\n\t" \
  "s_add_u32 m0, %[sx], 16384\n\t" \
  "s_nop 0\n\t" \
  "global_load_lds_dwordx4 %[vx0], s[44:45]\n\t" \
  "s_add_u32 m0, %[sx], 17408\n\t" \
  "s_nop 0\n\t" \
  "global_load_lds_dwordx4 %[vx1], s[44:45]\n\t" \
  "s_add_u32 s52, s52, 1\n\t" \
  "s_and_b32 s52, s52, 7\n\t" \
  "s_mul_i32 s44, s53, 196608\n\t" \
  "s_add_u32 s44, s42, s44\n\t" \
  "s_addc_u32 s45, s43, 0\n\t" \
  "s_add_u32 m0, %[sq], 77824\n\t" \
  "s_nop 0\n\t" \
  "global_load_lds_dwordx4 %[vq0], s[44:45]\n\t" \
  "s_add_u32 s53, s53, 1\n\t" \
  "s_and_b32 s53, s53, 7\n\t" \
  "s_waitcnt vmcnt(3)\n\t" \
  "s_barrier\n\t" \
  "ds_read_b128 v[128:131], %[xa] offset:0\n\t" \
  "ds_read_b64_tr_b16 v[48:49], %[wq0] offset:0\n\t" \
  "ds_read_b64_tr_b16 v[50:51], %[wq0] offset:768\n\t" \
  "ds_read_b64_tr_b16 v[52:53], %[wq1] offset:0\n\t" \
  "ds_read_b64_tr_b16 v[54:55], %[wq1] offset:768\n\t" \
  "ds_read_b128 v[132:135], v80 offset:0\n\t" \
  "ds_read_b64_tr_b16 v[56:57], %[wq0] offset:3072\n\t" \
  "ds_read_b64_tr_b16 v[58:59], %[wq0] offset:3840\n\t" \
  "ds_read_b64_tr_b16 v[60:61], %[wq1] offset:3072\n\t" \
  "ds_read_b64_tr_b16 v[62:63], %[wq1] offset:3840\n\t" \
  "ds_read_b128 v[136:139], v81 offset:0\n\t" \
  "ds_read_b64_tr_b16 v[64:65], %[wq0] offset:6144\n\t" \
  "ds_read_b64_tr_b16 v[66:67], %[wq0] offset:6912\n\t" \
  "ds_read_b64_tr_b16 v[68:69], %[wq1] offset:6144\n\t" \
  "ds_read_b64_tr_b16 v[70:71], %[wq1] offset:6912\n\t" \
  "s_mul_i32 s44, s52, 128\n\t" \
  "s_add_u32 s44, s40, s44\n\t" \
  "s_addc_u32 s45, s41, 0\n\t" \
  "s_add_u32 m0, %[sx], 32768\n\t" \
  "s_nop 0\n\t" \
  "global_load_lds_dwordx4 %[vx0], s[44:45]\n\t" \
  "s_waitcnt lgkmcnt(10)\n\t" \
  "v_mfma_f32_32x32x16_bf16 v[0:15], v[48:51], v[128:131], 0\n\t" \
  "v_mfma_f32_32x32x16_bf16 v[16:31], v[52:55], v[128:131], 0\n\t" \
  "ds_read_b128 v[140:143], v82 offset:0\n\t" \
  "ds_read_b64_tr_b16 v[48:49], %[wq0] offset:9216\n\t" \
  "ds_read_b64_tr_b16 v[50:51], %[wq0] offset:9984\n\t" \
  "ds_read_b64_tr_b16 v[52:53], %[wq1] offset:9216\n\t" \
  "ds_read_b64_tr_b16 v[54:55], %[wq1] offset:9984\n\t" \
  "s_add_u32 m0, %[sx], 33792\n\t" \
  "s_nop 0\n\t" \
  "global_load_lds_dwordx4 %[vx1], s[44:45]\n\t" \
  "s_add_u32 s52, s52, 1\n\t" \
  "s_and_b32 s52, s52, 7\n\t" \
  "s_waitcnt lgkmcnt(10)\n\t" \
  "v_mfma_f32_32x32x16_bf16 v[0:15], v[56:59], v[132:135], v[0:15]\n\t" \
  "v_mfma_f32_32x32x16_bf16 v[16:31], v[60:63], v[132:135], v[16:31]\n\t" \
  "s_waitcnt vmcnt(2)\n\t" \
  "s_barrier\n\t" \
  "ds_read_b128 v[144:147], %[xa] offset:16384\n\t" \
  "ds_read_b64_tr_b16 v[56:57], %[wq0] offset:12288\n\t" \
  "ds_read_b64_tr_b16 v[58:59], %[wq0] offset:13056\n\t" \
  "ds_read_b64_tr_b16 v[60:61], %[wq1] offset:12288\n\t" \
  "ds_read_b64_tr_b16 v[62:63], %[wq1] offset:13056\n\t" \
  "s_mul_i32 s44, s53, 196608\n\t" \
  "s_add_u32 s44, s42, s44\n\t" \
  "s_addc_u32 s45, s43, 0\n\t" \
  "s_add_u32 m0, %[sq], 90112\n\t" \
  "s_nop 0\n\t" \
  "global_load_lds_dwordx4 %[vq0], s[44:45]\n\t" \
  "s_add_u32 s53, s53, 1\n\t" \
  "s_and_b32 s53, s53, 7\n\t" \
  "s_waitcnt lgkmcnt(10)\n\t" \
  "v_mfma_f32_32x32x16_bf16 v[0:15], v[64:67], v[136:139], v[0:15]\n\t" \
  "v_mfma_f32_32x32x16_bf16 v[16:31], v[68:71], v[136:139], v[16:31]\n\t" \
  "ds_read_b128 v[148:151], v80 offset:16384\n\t" \
  "ds_read_b64_tr_b16 v[64:65], %[wq0] offset:15360\n\t" \
  "ds_read_b64_tr_b16 v[66:67], %[wq0] offset:16128\n\t" \
  "ds_read_b64_tr_b16 v[68:69], %[wq1] offset:15360\n\t" \
  "ds_read_b64_tr_b16 v[70:71], %[wq1] offset:16128\n\t" \
  "s_mul_i32 s44, s52, 128\n\t" \
  "s_add_u32 s44, s40, s44\n\t" \
  "s_addc_u32 s45, s41, 0\n\t" \
  "s_add_u32 m0, %[sx], 49152\n\t" \
  "s_nop 0\n\t" \
  "global_load_lds_dwordx4 %[vx0], s[44:45]\n\t" \
  "s_waitcnt lgkmcnt(10)\n\t" \
  "v_mfma_f32_32x32x16_bf16 v[0:15], v[48:51], v[140:143], v[0:15]\n\t" \
  "v_mfma_f32_32x32x16_bf16 v[16:31], v[52:55], v[140:143], v[16:31]\n\t" \
  "ds_read_b128 v[152:155], v81 offset:16384\n\t" \
  "ds_read_b64_tr_b16 v[48:49], %[wq0] offset:18432\n\t" \
  "ds_read_b64_tr_b16 v[50:51], %[wq0] offset:19200\n\t" \
  "ds_read_b64_tr_b16 v[52:53], %[wq1] offset:18432\n\t" \
  "ds_read_b64_tr_b16 v[54:55], %[wq1] offset:19200\n\t" \
  "s_add_u32 m0, %[sx], 50176\n\t" \
  "s_nop 0\n\t" \
  "global_load_lds_dwordx4 %[vx1], s[44:45]\n\t" \
  "s_add_u32 s52, s52, 1\n\t" \
  "s_and_b32 s52, s52, 7\n\t" \
  "s_waitcnt lgkmcnt(10)\n\t" \
  "v_mfma_f32_32x32x16_bf16 v[0:15], v[56:59], v[144:147], v[0:15]\n\t" \
  "v_mfma_f32_32x32x16_bf16 v[16:31], v[60:63], v[144:147], v[16:31]\n\t" \
  "ds_read_b128 v[156:159], v82 offset:16384\n\t" \
  "ds_read_b64_tr_b16 v[56:57], %[wq0] offset:21504\n\t" \
  "ds_read_b64_tr_b16 v[58:59], %[wq0] offset:22272\n\t" \
  "ds_read_b64_tr_b16 v[60:61], %[wq1] offset:21504\n\t" \
  "ds_read_b64_tr_b16 v[62:63], %[wq1] offset:22272\n\t" \
  "s_mul_i32 s44, s53, 196608\n\t" \
  "s_add_u32 s44, s42, s44\n\t" \
  "s_addc_u32 s45, s43, 0\n\t" \
  "s_add_u32 m0, %[sq], 102400\n\t" \
  "s_nop 0\n\t" \
  "global_load_lds_dwordx4 %[vq0], s[44:45]\n\t" \
  "s_add_u32 s53, s53, 1\n\t" \
  "s_and_b32 s53, s53, 7\n\t" \
  "s_waitcnt lgkmcnt(10)\n\t" \
  "v_mfma_f32_32x32x16_bf16 v[0:15], v[64:67], v[148:151], v[0:15]\n\t" \
  "v_mfma_f32_32x32x16_bf16 v[16:31], v[68:71], v[148:151], v[16:31]\n\t" \
  "s_waitcnt vmcnt(3)\n\t" \
  "s_barrier\n\t" \
  "ds_read_b128 v[160:163], %[xa] offset:32768\n\t" \
  "ds_read_b64_tr_b16 v[64:65], %[wq0] offset:24576\n\t" \
  "ds_read_b64_tr_b16 v[66:67], %[wq0] offset:25344\n\t" \
  "ds_read_b64_tr_b16 v[68:69], %[wq1] offset:24576\n\t" \
  "ds_read_b64_tr_b16 v[70:71], %[wq1] offset:25344\n\t" \
  "s_mul_i32 s44, s53, 196608\n\t" \
  "s_add_u32 s44, s42, s44\n\t" \
  "s_addc_u32 s45, s43, 0\n\t" \
  "s_add_u32 m0, %[sq], 65536\n\t" \
  "s_nop 0\n\t" \
  "global_load_lds_dwordx4 %[vq0], s[44:45]\n\t" \
  "s_add_u32 s53, s53, 1\n\t" \
  "s_and_b32 s53, s53, 7\n\t" \
  "s_waitcnt lgkmcnt(10)\n\t" \
  "v_mfma_f32_32x32x16_bf16 v[0:15], v[48:51], v[152:155], v[0:15]\n\t" \
  "v_mfma_f32_32x32x16_bf16 v[16:31], v[52:55], v[152:155], v[16:31]\n\t" \
  "ds_read_b128 v[164:167], v80 offset:32768\n\t" \
  "ds_read_b64_tr_b16 v[48:49], %[wq0] offset:27648\n\t" \
  "ds_read_b64_tr_b16 v[50:51], %[wq0] offset:28416\n\t" \
  "ds_read_b64_tr_b16 v[52:53], %[wq1] offset:27648\n\t" \
  "ds_read_b64_tr_b16 v[54:55], %[wq1] offset:28416\n\t" \
  "s_mul_i32 s44, s52, 128\n\t" \
  "s_add_u32 s44, s40, s44\n\t" \
  "s_addc_u32 s45, s41, 0\n\t" \
  "s_add_u32 m0, %[sx], 0\n\t" \
  "s_nop 0\n\t" \
  "global_load_lds_dwordx4 %[vx0], s[44:45]\n\t" \
  "s_waitcnt lgkmcnt(10)\n\t" \
  "v_mfma_f32_32x32x16_bf16 v[0:15], v[56:59], v[156:159], v[0:15]\n\t" \
  "v_mfma_f32_32x32x16_bf16 v[16:31], v[60:63], v[156:159], v[16:31]\n\t" \
  "ds_read_b128 v[168:171], v81 offset:32768\n\t" \
  "ds_read_b64_tr_b16 v[56:57], %[wq0] offset:30720\n\t" \
  "ds_read_b64_tr_b16 v[58:59], %[wq0] offset:31488\n\t" \
  "ds_read_b64_tr_b16 v[60:61], %[wq1] offset:30720\n\t" \
  "ds_read_b64_tr_b16 v[62:63], %[wq1] offset:31488\n\t" \
  "s_add_u32 m0, %[sx], 1024\n\t" \
  "s_nop 0\n\t" \
  "global_load_lds_dwordx4 %[vx1], s[44:45]\n\t" \
  "s_add_u32 s52, s52, 1\n\t" \
  "s_and_b32 s52, s52, 7\n\t" \
  "s_waitcnt lgkmcnt(10)\n\t" \
  "v_mfma_f32_32x32x16_bf16 v[0:15], v[64:67], v[160:163], v[0:15]\n\t" \
  "v_mfma_f32_32x32x16_bf16 v[16:31], v[68:71], v[160:163], v[16:31]\n\t" \
  "ds_read_b128 v[172:175], v82 offset:32768\n\t" \
  "ds_read_b64_tr_b16 v[64:65], %[wq0] offset:33792\n\t" \
  "ds_read_b64_tr_b16 v[66:67], %[wq0] offset:34560\n\t" \
  "ds_read_b64_tr_b16 v[68:69], %[wq1] offset:33792\n\t" \
  "ds_read_b64_tr_b16 v[70:71], %[wq1] offset:34560\n\t" \
  "global_load_dword v84, %[pfo], s[46:47]\n\t" \
  "s_waitcnt lgkmcnt(10)\n\t" \
  "v_mfma_f32_32x32x16_bf16 v[0:15], v[48:51], v[164:167], v[0:15]\n\t" \
  "v_mfma_f32_32x32x16_bf16 v[16:31], v[52:55], v[164:167], v[16:31]\n\t" \
  "s_waitcnt vmcnt(4)\n\t" \
  "s_barrier\n\t" \
  "ds_read_b128 v[176:179], %[xa] offset:49152\n\t" \
  "ds_read_b64_tr_b16 v[48:49], %[wq0] offset:36864\n\t" \
  "ds_read_b64_tr_b16 v[50:51], %[wq0] offset:37632\n\t" \
  "ds_read_b64_tr_b16 v[52:53], %[wq1] offset:36864\n\t" \
  "ds_read_b64_tr_b16 v[54:55], %[wq1] offset:37632\n\t" \
  "s_mul_i32 s44, s53, 196608\n\t" \
  "s_add_u32 s44, s42, s44\n\t" \
  "s_addc_u32 s45, s43, 0\n\t" \
  "s_add_u32 m0, %[sq], 77824\n\t" \
  "s_nop 0\n\t" \
  "global_load_lds_dwordx4 %[vq0], s[44:45]\n\t" \
  "s_add_u32 s53, s53, 1\n\t" \
  "s_and_b32 s53, s53, 7\n\t" \
  "s_waitcnt lgkmcnt(10)\n\t" \
  "v_mfma_f32_32x32x16_bf16 v[0:15], v[56:59], v[168:171], v[0:15]\n\t" \
  "v_mfma_f32_32x32x16_bf16 v[16:31], v[60:63], v[168:171], v[16:31]\n\t" \
  "ds_read_b128 v[180:183], v80 offset:49152\n\t" \
  "ds_read_b64_tr_b16 v[56:57], %[wq0] offset:39936\n\t" \
  "ds_read_b64_tr_b16 v[58:59], %[wq0] offset:40704\n\t" \
  "ds_read_b64_tr_b16 v[60:61], %[wq1] offset:39936\n\t" \
  "ds_read_b64_tr_b16 v[62:63], %[wq1] offset:40704\n\t" \
  "s_mul_i32 s44, s52, 128\n\t" \
  "s_add_u32 s44, s40, s44\n\t" \
  "s_addc_u32 s45, s41, 0\n\t" \
  "s_add_u32 m0, %[sx], 16384\n\t" \
  "s_nop 0\n\t" \
  "global_load_lds_dwordx4 %[vx0], s[44:45]\n\t" \
  "s_waitcnt lgkmcnt(10)\n\t" \
  "v_mfma_f32_32x32x16_bf16 v[0:15], v[64:67], v[172:175], v[0:15]\n\t" \
  "v_mfma_f32_32x32x16_bf16 v[16:31], v[68:71], v[172:175], v[16:31]\n\t" \
  "ds_read_b128 v[184:187], v81 offset:49152\n\t" \
  "ds_read_b64_tr_b16 v[64:65], %[wq0] offset:43008\n\t" \
  "ds_read_b64_tr_b16 v[66:67], %[wq0] offset:43776\n\t" \
  "ds_read_b64_tr_b16 v[68:69], %[wq1] offset:43008\n\t" \
  "ds_read_b64_tr_b16 v[70:71], %[wq1] offset:43776\n\t" \
  "s_add_u32 m0, %[sx], 17408\n\t" \
  "s_nop 0\n\t" \
  "global_load_lds_dwordx4 %[vx1], s[44:45]\n\t" \
  "s_add_u32 s52, s52, 1\n\t" \
  "s_and_b32 s52, s52, 7\n\t" \
  "s_waitcnt lgkmcnt(10)\n\t" \
  "v_mfma_f32_32x32x16_bf16 v[0:15], v[48:51], v[176:179], v[0:15]\n\t" \
  "v_mfma_f32_32x32x16_bf16 v[16:31], v[52:55], v[176:179], v[16:31]\n\t" \
  "ds_read_b128 v[188:191], v82 offset:49152\n\t" \
  "ds_read_b64_tr_b16 v[48:49], %[wq0] offset:46080\n\t" \
  "ds_read_b64_tr_b16 v[50:51], %[wq0] offset:46848\n\t" \
  "ds_read_b64_tr_b16 v[52:53], %[wq1] offset:46080\n\t" \
  "ds_read_b64_tr_b16 v[54:55], %[wq1] offset:46848\n\t" \
  "s_waitcnt lgkmcnt(10)\n\t" \
  "v_mfma_f32_32x32x16_bf16 v[0:15], v[56:59], v[180:183], v[0:15]\n\t" \
  "v_mfma_f32_32x32x16_bf16 v[16:31], v[60:63], v[180:183], v[16:31]\n\t" \
  "s_waitcnt vmcnt(4)\n\t" \
  "s_barrier\n\t" \
  "ds_read_b128 v[192:195], %[xa] offset:0\n\t" \
  "ds_read_b64_tr_b16 v[56:57], %[wq0] offset:0\n\t" \
  "ds_read_b64_tr_b16 v[58:59], %[wq0] offset:768\n\t" \
  "ds_read_b64_tr_b16 v[60:61], %[wq1] offset:0\n\t" \
  "ds_read_b64_tr_b16 v[62:63], %[wq1] offset:768\n\t" \
  "s_mul_i32 s44, s53, 196608\n\t" \
  "s_add_u32 s44, s42, s44\n\t" \
  "s_addc_u32 s45, s43, 0\n\t" \
  "s_add_u32 m0, %[sq], 90112\n\t" \
  "s_nop 0\n\t" \
  "global_load_lds_dwordx4 %[vq0], s[44:45]\n\t" \
  "s_add_u32 s53, s53, 1\n\t" \
  "s_and_b32 s53, s53, 7\n\t" \
  "s_waitcnt lgkmcnt(10)\n\t" \
  "v_mfma_f32_32x32x16_bf16 v[0:15], v[64:67], v[184:187], v[0:15]\n\t" \
  "v_mfma_f32_32x32x16_bf16 v[16:31], v[68:71], v[184:187], v[16:31]\n\t" \
  "ds_read_b128 v[196:199], v80 offset:0\n\t" \
  "ds_read_b64_tr_b16 v[64:65], %[wq0] offset:3072\n\t" \
  "ds_read_b64_tr_b16 v[66:67], %[wq0] offset:3840\n\t" \
  "ds_read_b64_tr_b16 v[68:69], %[wq1] offset:3072\n\t" \
  "ds_read_b64_tr_b16 v[70:71], %[wq1] offset:3840\n\t" \
  "s_mul_i32 s44, s52, 128\n\t" \
  "s_add_u32 s44, s40, s44\n\t" \
  "s_addc_u32 s45, s41, 0\n\t" \
  "s_add_u32 m0, %[sx], 32768\n\t" \
  "s_nop 0\n\t" \
  "global_load_lds_dwordx4 %[vx0], s[44:45]\n\t" \
  "s_waitcnt lgkmcnt(10)\n\t" \
  "v_mfma_f32_32x32x16_bf16 v[0:15], v[48:51], v[188:191], v[0:15]\n\t" \
  "v_mfma_f32_32x32x16_bf16 v[16:31], v[52:55], v[188:191], v[16:31]\n\t" \
  "ds_read_b128 v[200:203], v81 offset:0\n\t" \
  "ds_read_b64_tr_b16 v[48:49], %[wq0] offset:6144\n\t" \
  "ds_read_b64_tr_b16 v[50:51], %[wq0] offset:6912\n\t" \
  "ds_read_b64_tr_b16 v[52:53], %[wq1] offset:6144\n\t" \
  "ds_read_b64_tr_b16 v[54:55], %[wq1] offset:6912\n\t" \
  "s_add_u32 m0, %[sx], 33792\n\t" \
  "s_nop 0\n\t" \
  "global_load_lds_dwordx4 %[vx1], s[44:45]\n\t" \
  "s_add_u32 s52, s52, 1\n\t" \
  "s_and_b32 s52, s52, 7\n\t" \
  "s_waitcnt lgkmcnt(10)\n\t" \
  "v_mfma_f32_32x32x16_bf16 v[0:15], v[56:59], v[192:195], v[0:15]\n\t" \
  "v_mfma_f32_32x32x16_bf16 v[16:31], v[60:63], v[192:195], v[16:31]\n\t" \
  "ds_read_b128 v[204:207], v82 offset:0\n\t" \
  "ds_read_b64_tr_b16 v[56:57], %[wq0] offset:9216\n\t" \
  "ds_read_b64_tr_b16 v[58:59], %[wq0] offset:9984\n\t" \
  "ds_read_b64_tr_b16 v[60:61], %[wq1] offset:9216\n\t" \
  "ds_read_b64_tr_b16 v[62:63], %[wq1] offset:9984\n\t" \
  "s_waitcnt lgkmcnt(10)\n\t" \
  "v_mfma_f32_32x32x16_bf16 v[0:15], v[64:67], v[196:199], v[0:15]\n\t" \
  "v_mfma_f32_32x32x16_bf16 v[16:31], v[68:71], v[196:199], v[16:31]\n\t" \
  "s_waitcnt vmcnt(3)\n\t" \
  "s_barrier\n\t" \
  "ds_read_b128 v[208:211], %[xa] offset:16384\n\t" \
  "ds_read_b64_tr_b16 v[64:65], %[wq0] offset:12288\n\t" \
  "ds_read_b64_tr_b16 v[66:67], %[wq0] offset:13056\n\t" \
  "ds_read_b64_tr_b16 v[68:69], %[wq1] offset:12288\n\t" \
  "ds_read_b64_tr_b16 v[70:71], %[wq1] offset:13056\n\t" \
  "s_mul_i32 s44, s53, 196608\n\t" \
  "s_add_u32 s44, s42, s44\n\t" \
  "s_addc_u32 s45, s43, 0\n\t" \
  "s_add_u32 m0, %[sq], 102400\n\t" \
  "s_nop 0\n\t" \
  "global_load_lds_dwordx4 %[vq0], s[44:45]\n\t" \
  "s_add_u32 s53, s53, 1\n\t" \
  "s_and_b32 s53, s53, 7\n\t" \
  "s_waitcnt lgkmcnt(10)\n\t" \
  "v_mfma_f32_32x32x16_bf16 v[0:15], v[48:51], v[200:203], v[0:15]\n\t" \
  "v_mfma_f32_32x32x16_bf16 v[16:31], v[52:55], v[200:203], v[16:31]\n\t" \
  "ds_read_b128 v[212:215], v80 offset:16384\n\t" \
  "ds_read_b64_tr_b16 v[48:49], %[wq0] offset:15360\n\t" \
  "ds_read_b64_tr_b16 v[50:51], %[wq0] offset:16128\n\t" \
  "ds_read_b64_tr_b16 v[52:53], %[wq1] offset:15360\n\t" \
  "ds_read_b64_tr_b16 v[54:55], %[wq1] offset:16128\n\t" \
  "s_mul_i32 s44, s52, 128\n\t" \
  "s_add_u32 s44, s40, s44\n\t" \
  "s_addc_u32 s45, s41, 0\n\t" \
  "s_add_u32 m0, %[sx], 49152\n\t" \
  "s_nop 0\n\t" \
  "global_load_lds_dwordx4 %[vx0], s[44:45]\n\t" \
  "s_waitcnt lgkmcnt(10)\n\t" \
  "v_mfma_f32_32x32x16_bf16 v[0:15], v[56:59], v[204:207], v[0:15]\n\t" \
  "v_mfma_f32_32x32x16_bf16 v[16:31], v[60:63], v[204:207], v[16:31]\n\t" \
  "ds_read_b128 v[216:219], v81 offset:16384\n\t" \
  "ds_read_b64_tr_b16 v[56:57], %[wq0] offset:18432\n\t" \
  "ds_read_b64_tr_b16 v[58:59], %[wq0] offset:19200\n\t" \
  "ds_read_b64_tr_b16 v[60:61], %[wq1] offset:18432\n\t" \
  "ds_read_b64_tr_b16 v[62:63], %[wq1] offset:19200\n\t" \
  "s_add_u32 m0, %[sx], 50176\n\t" \
  "s_nop 0\n\t" \
  "global_load_lds_dwordx4 %[vx1], s[44:45]\n\t" \
  "s_add_u32 s52, s52, 1\n\t" \
  "s_and_b32 s52, s52, 7\n\t" \
  "s_waitcnt lgkmcnt(10)\n\t" \
  "v_mfma_f32_32x32x16_bf16 v[0:15], v[64:67], v[208:211], v[0:15]\n\t" \
  "v_mfma_f32_32x32x16_bf16 v[16:31], v[68:71], v[208:211], v[16:31]\n\t" \
  "ds_read_b128 v[220:223], v82 offset:16384\n\t" \
  "ds_read_b64_tr_b16 v[64:65], %[wq0] offset:21504\n\t" \
  "ds_read_b64_tr_b16 v[66:67], %[wq0] offset:22272\n\t" \
  "ds_read_b64_tr_b16 v[68:69], %[wq1] offset:21504\n\t" \
  "ds_read_b64_tr_b16 v[70:71], %[wq1] offset:22272\n\t" \
  "s_waitcnt lgkmcnt(10)\n\t" \
  "v_mfma_f32_32x32x16_bf16 v[0:15], v[48:51], v[212:215], v[0:15]\n\t" \
  "v_mfma_f32_32x32x16_bf16 v[16:31], v[52:55], v[212:215], v[16:31]\n\t" \
  "s_waitcnt vmcnt(3)\n\t" \
  "s_barrier\n\t" \
  "ds_read_b128 v[224:227], %[xa] offset:32768\n\t" \
  "ds_read_b64_tr_b16 v[48:49], %[wq0] offset:24576\n\t" \
  "ds_read_b64_tr_b16 v[50:51], %[wq0] offset:25344\n\t" \
  "ds_read_b64_tr_b16 v[52:53], %[wq1] offset:24576\n\t" \
  "ds_read_b64_tr_b16 v[54:55], %[wq1] offset:25344\n\t" \
  "s_mul_i32 s44, %[fi], 176128\n\t" \
  "s_add_u32 s44, s46, s44\n\t" \
  "s_addc_u32 s45, s47, 0\n\t" \
  "s_add_u32 m0, %[sw], 114688\n\t" \
  "s_nop 0\n\t" \
  "global_load_lds_dwordx4 %[vf0], s[44:45]\n\t" \
  "s_waitcnt lgkmcnt(10)\n\t" \
  "v_mfma_f32_32x32x16_bf16 v[0:15], v[56:59], v[216:219], v[0:15]\n\t" \
  "v_mfma_f32_32x32x16_bf16 v[16:31], v[60:63], v[216:219], v[16:31]\n\t" \
  "ds_read_b128 v[228:231], v80 offset:32768\n\t" \
  "ds_read_b64_tr_b16 v[56:57], %[wq0] offset:27648\n\t" \
  "ds_read_b64_tr_b16 v[58:59], %[wq0] offset:28416\n\t" \
  "ds_read_b64_tr_b16 v[60:61], %[wq1] offset:27648\n\t" \
  "ds_read_b64_tr_b16 v[62:63], %[wq1] offset:28416\n\t" \
  "s_add_u32 m0, %[sw], 115712\n\t" \
  "s_nop 0\n\t" \
  "global_load_lds_dwordx4 %[vf1], s[44:45]\n\t" \
  "s_waitcnt lgkmcnt(10)\n\t" \
  "v_mfma_f32_32x32x16_bf16 v[0:15], v[64:67], v[220:223], v[0:15]\n\t" \
  "v_mfma_f32_32x32x16_bf16 v[16:31], v[68:71], v[220:223], v[16:31]\n\t" \
  "ds_read_b128 v[232:235], v81 offset:32768\n\t" \
  "ds_read_b64_tr_b16 v[64:65], %[wq0] offset:30720\n\t" \
  "ds_read_b64_tr_b16 v[66:67], %[wq0] offset:31488\n\t" \
  "ds_read_b64_tr_b16 v[68:69], %[wq1] offset:30720\n\t" \
  "ds_read_b64_tr_b16 v[70:71], %[wq1] offset:31488\n\t" \
  "s_add_u32 m0, %[sw], 116736\n\t" \
  "s_nop 0\n\t" \
  "global_load_lds_dwordx4 %[vf2], s[44:45]\n\t" \
  "s_add_u32 %[fi], %[fi], 1\n\t" \
  "s_and_b32 %[fi], %[fi], 7\n\t" \
  "s_waitcnt lgkmcnt(10)\n\t" \
  "v_mfma_f32_32x32x16_bf16 v[0:15], v[48:51], v[224:227], v[0:15]\n\t" \
  "v_mfma_f32_32x32x16_bf16 v[16:31], v[52:55], v[224:227], v[16:31]\n\t" \
  "ds_read_b128 v[236:239], v82 offset:32768\n\t" \
  "ds_read_b64_tr_b16 v[48:49], %[wq0] offset:33792\n\t" \
  "ds_read_b64_tr_b16 v[50:51], %[wq0] offset:34560\n\t" \
  "ds_read_b64_tr_b16 v[52:53], %[wq1] offset:33792\n\t" \
  "ds_read_b64_tr_b16 v[54:55], %[wq1] offset:34560\n\t" \
  "s_waitcnt lgkmcnt(10)\n\t" \
  "v_mfma_f32_32x32x16_bf16 v[0:15], v[56:59], v[228:231], v[0:15]\n\t" \
  "v_mfma_f32_32x32x16_bf16 v[16:31], v[60:63], v[228:231], v[16:31]\n\t" \
  "s_waitcnt vmcnt(3)\n\t" \
  "s_barrier\n\t" \
  "ds_read_b128 v[240:243], %[xa] offset:49152\n\t" \
  "ds_read_b64_tr_b16 v[56:57], %[wq0] offset:36864\n\t" \
  "ds_read_b64_tr_b16 v[58:59], %[wq0] offset:37632\n\t" \
  "ds_read_b64_tr_b16 v[60:61], %[wq1] offset:36864\n\t" \
  "ds_read_b64_tr_b16 v[62:63], %[wq1] offset:37632\n\t" \
  "s_mul_i32 s44, %[fi], 176128\n\t" \
  "s_add_u32 s44, s46, s44\n\t" \
  "s_addc_u32 s45, s47, 0\n\t" \
  "s_add_u32 m0, %[sw], 65536\n\t" \
  "s_nop 0\n\t" \
  "global_load_lds_dwordx4 %[vf0], s[44:45]\n\t" \
  "s_waitcnt lgkmcnt(10)\n\t" \
  "v_mfma_f32_32x32x16_bf16 v[0:15], v[64:67], v[232:235], v[0:15]\n\t" \
  "v_mfma_f32_32x32x16_bf16 v[16:31], v[68:71], v[232:235], v[16:31]\n\t" \
  "ds_read_b128 v[244:247], v80 offset:49152\n\t" \
  "ds_read_b64_tr_b16 v[64:65], %[wq0] offset:39936\n\t" \
  "ds_read_b64_tr_b16 v[66:67], %[wq0] offset:40704\n\t" \
  "ds_read_b64_tr_b16 v[68:69], %[wq1] offset:39936\n\t" \
  "ds_read_b64_tr_b16 v[70:71], %[wq1] offset:40704\n\t" \
  "s_add_u32 m0, %[sw], 66560\n\t" \
  "s_nop 0\n\t" \
  "global_load_lds_dwordx4 %[vf1], s[44:45]\n\t" \
  "s_waitcnt lgkmcnt(10)\n\t" \
  "v_mfma_f32_32x32x16_bf16 v[0:15], v[48:51], v[236:239], v[0:15]\n\t" \
  "v_mfma_f32_32x32x16_bf16 v[16:31], v[52:55], v[236:239], v[16:31]\n\t" \
  "ds_read_b128 v[248:251], v81 offset:49152\n\t" \
  "ds_read_b64_tr_b16 v[48:49], %[wq0] offset:43008\n\t" \
  "ds_read_b64_tr_b16 v[50:51], %[wq0] offset:43776\n\t" \
  "ds_read_b64_tr_b16 v[52:53], %[wq1] offset:43008\n\t" \
  "ds_read_b64_tr_b16 v[54:55], %[wq1] offset:43776\n\t" \
  "s_add_u32 m0, %[sw], 67584\n\t" \
  "s_nop 0\n\t" \
  "global_load_lds_dwordx4 %[vf2], s[44:45]\n\t" \
  "s_add_u32 %[fi], %[fi], 1\n\t" \
  "s_and_b32 %[fi], %[fi], 7\n\t" \
  "s_waitcnt lgkmcnt(10)\n\t" \
  "v_mfma_f32_32x32x16_bf16 v[0:15], v[56:59], v[240:243], v[0:15]\n\t" \
  "v_mfma_f32_32x32x16_bf16 v[16:31], v[60:63], v[240:243], v[16:31]\n\t" \
  "ds_read_b128 v[252:255], v82 offset:49152\n\t" \
  "ds_read_b64_tr_b16 v[56:57], %[wq0] offset:46080\n\t" \
  "ds_read_b64_tr_b16 v[58:59], %[wq0] offset:46848\n\t" \
  "ds_read_b64_tr_b16 v[60:61], %[wq1] offset:46080\n\t" \
  "ds_read_b64_tr_b16 v[62:63], %[wq1] offset:46848\n\t" \
  "s_mul_i32 s44, %[fi], 176128\n\t" \
  "s_add_u32 s44, s46, s44\n\t" \
  "s_addc_u32 s45, s47, 0\n\t" \
  "s_add_u32 m0, %[sw], 0\n\t" \
  "s_nop 0\n\t" \
  "global_load_lds_dwordx4 %[vf0], s[44:45]\n\t" \
  "s_waitcnt lgkmcnt(10)\n\t" \
  "v_mfma_f32_32x32x16_bf16 v[0:15], v[64:67], v[244:247], v[0:15]\n\t" \
  "v_mfma_f32_32x32x16_bf16 v[16:31], v[68:71], v[244:247], v[16:31]\n\t" \
  "s_add_u32 m0, %[sw], 1024\n\t" \
  "s_nop 0\n\t" \
  "global_load_lds_dwordx4 %[vf1], s[44:45]\n\t" \
  "s_waitcnt lgkmcnt(5)\n\t" \
  "v_mfma_f32_32x32x16_bf16 v[0:15], v[48:51], v[248:251], v[0:15]\n\t" \
  "v_mfma_f32_32x32x16_bf16 v[16:31], v[52:55], v[248:251], v[16:31]\n\t" \
  "s_add_u32 m0, %[sw], 2048\n\t" \
  "s_nop 0\n\t" \
  "global_load_lds_dwordx4 %[vf2], s[44:45]\n\t" \
  "s_add_u32 %[fi], %[fi], 1\n\t" \
  "s_and_b32 %[fi], %[fi], 7\n\t" \
  "s_waitcnt lgkmcnt(0)\n\t" \
  "v_mfma_f32_32x32x16_bf16 v[0:15], v[56:59], v[252:255], v[0:15]\n\t" \
  "v_mfma_f32_32x32x16_bf16 v[16:31], v[60:63], v[252:255], v[16:31]\n\t" \
  "s_mov_b32 m0, %[m0s]\n\t" \
  "s_nop 15\n\t" \
  ""
#define P1_ASM_1 \
  "s_mov_b32 %[m0s], m0\n\t" \
  "v_mov_b32 v83, 0\n\t" \
  "global_load_dwordx2 %[ep], v83, %[epp] sc1\n\t" \
  "s_mov_b32 s52, %[fi]\n\t" \
  "s_mov_b32 s53, %[fi]\n\t" \
  "v_xor_b32 v80, 32, %[xa]\n\t" \
  "v_xor_b32 v81, 64, %[xa]\n\t" \
  "v_xor_b32 v82, 0x60, %[xa]\n\t" \
  "s_mul_i32 s44, s52, 128\n\t" \
  "s_add_u32 s44, s40, s44\n\t" \
  "s_addc_u32 s45, s41, 0\n\t" \
  "s_add_u32 m0, %[sx], 0\n\t" \
  "s_nop 0\n\t" \
  "global_load_lds_dwordx4 %[vx0], s[44:45]\n\t" \
  "s_add_u32 m0, %[sx], 1024\n\t" \
  "s_nop 0\n\t" \
  "global_load_lds_dwordx4 %[vx1], s[44:45]\n\t" \
  "s_add_u32 s52, s52, 1\n\t" \
  "s_and_b32 s52, s52, 7\n\t" \
  "s_mul_i32 s44, s53, 196608\n\t" \
  "s_add_u32 s44, s42, s44\n\t" \
  "s_addc_u32 s45, s43, 0\n\t" \
  "s_add_u32 m0, %[sq], 65536\n\t" \
  "s_nop 0\n\t" \
  "global_load_lds_dwordx4 %[vq0], s[44:45]\n\t" \
  "s_add_u32 m0, %[sq], 66560\n\t" \
  "s_nop 0\n\t" \
  "global_load_lds_dwordx4 %[vq1], s[44:45]\n\t" \
  "s_add_u32 s53, s53, 1\n\t" \
  "s_and_b32 s53, s53, 7\n\t" \
  "s_mul_i32 s44, s52, 128\n\t" \
  "s_add_u32 s44, s40, s44\n\t" \
  "s_addc_u32 s45, s41, 0\n\t" \
  "s_add_u32 m0, %[sx], 16384\n\t" \
  "s_nop 0\n\t" \
  "global_load_lds_dwordx4 %[vx0], s[44:45]\n\t" \
  "s_add_u32 m0, %[sx], 17408\n\t" \
  "s_nop 0\n\t" \
  "global_load_lds_dwordx4 %[vx1], s[44:45]\n\t" \
  "s_add_u32 s52, s52, 1\n\t" \
  "s_and_b32 s52, s52, 7\n\t" \
  "s_mul_i32 s44, s53, 196608\n\t" \
  "s_add_u32 s44, s42, s44\n\t" \
  "s_addc_u32 s45, s43, 0\n\t" \
  "s_add_u32 m0, %[sq], 77824\n\t" \
  "s_nop 0\n\t" \
  "global_load_lds_dwordx4 %[vq0], s[44:45]\n\t" \
  "s_add_u32 m0, %[sq], 78848\n\t" \
  "s_nop 0\n\t" \
  "global_load_lds_dwordx4 %[vq1], s[44:45]\n\t" \
  "s_add_u32 s53, s53, 1\n\t" \
  "s_and_b32 s53, s53, 7\n\t" \
  "s_waitcnt vmcnt(4)\n\t" \
  "s_barrier\n\t" \
  "ds_read_b128 v[128:131], %[xa] offset:0\n\t" \
  "ds_read_b64_tr_b16 v[48:49], %[wq0] offset:0\n\t" \
  "ds_read_b64_tr_b16 v[50:51], %[wq0] offset:768\n\t" \
  "ds_read_b128 v[132:135], v80 offset:0\n\t" \
  "ds_read_b64_tr_b16 v[52:53], %[wq0] offset:3072\n\t" \
  "ds_read_b64_tr_b16 v[54:55], %[wq0] offset:3840\n\t" \
  "ds_read_b128 v[136:139], v81 offset:0\n\t" \
  "ds_read_b64_tr_b16 v[56:57], %[wq0] offset:6144\n\t" \
  "ds_read_b64_tr_b16 v[58:59], %[wq0] offset:6912\n\t" \
  "s_mul_i32 s44, s52, 128\n\t" \
  "s_add_u32 s44, s40, s44\n\t" \
  "s_addc_u32 s45, s41, 0\n\t" \
  "s_add_u32 m0, %[sx], 32768\n\t" \
  "s_nop 0\n\t" \
  "global_load_lds_dwordx4 %[vx0], s[44:45]\n\t" \
  "s_add_u32 m0, %[sx], 33792\n\t" \
  "s_nop 0\n\t" \
  "global_load_lds_dwordx4 %[vx1], s[44:45]\n\t" \
  "s_add_u32 s52, s52, 1\n\t" \
  "s_and_b32 s52, s52, 7\n\t" \
  "s_waitcnt lgkmcnt(6)\n\t" \
  "v_mfma_f32_32x32x16_bf16 v[0:15], v[48:51], v[128:131], 0\n\t" \
  "ds_read_b128 v[140:143], v82 offset:0\n\t" \
  "ds_read_b64_tr_b16 v[48:49], %[wq0] offset:9216\n\t" \
  "ds_read_b64_tr_b16 v[50:51], %[wq0] offset:9984\n\t" \
  "s_mul_i32 s44, s53, 196608\n\t" \
  "s_add_u32 s44, s42, s44\n\t" \
  "s_addc_u32 s45, s43, 0\n\t" \
  "s_add_u32 m0, %[sq], 90112\n\t" \
  "s_nop 0\n\t" \
  "global_load_lds_dwordx4 %[vq0], s[44:45]\n\t" \
  "s_waitcnt lgkmcnt(6)\n\t" \
  "v_mfma_f32_32x32x16_bf16 v[0:15], v[52:55], v[132:135], v[0:15]\n\t" \
  "s_waitcnt vmcnt(3)\n\t" \
  "s_barrier\n\t" \
  "ds_read_b128 v[144:147], %[xa] offset:16384\n\t" \
  "ds_read_b64_tr_b16 v[52:53], %[wq0] offset:12288\n\t" \
  "ds_read_b64_tr_b16 v[54:55], %[wq0] offset:13056\n\t" \
  "s_add_u32 m0, %[sq], 91136\n\t" \
  "s_nop 0\n\t" \
  "global_load_lds_dwordx4 %[vq1], s[44:45]\n\t" \
  "s_add_u32 s53, s53, 1\n\t" \
  "s_and_b32 s53, s53, 7\n\t" \
  "s_waitcnt lgkmcnt(6)\n\t" \
  "v_mfma_f32_32x32x16_bf16 v[0:15], v[56:59], v[136:139], v[0:15]\n\t" \
  "ds_read_b128 v[148:151], v80 offset:16384\n\t" \
  "ds_read_b64_tr_b16 v[56:57], %[wq0] offset:15360\n\t" \
  "ds_read_b64_tr_b16 v[58:59], %[wq0] offset:16128\n\t" \
  "s_mul_i32 s44, s52, 128\n\t" \
  "s_add_u32 s44, s40, s44\n\t" \
  "s_addc_u32 s45, s41, 0\n\t" \
  "s_add_u32 m0, %[sx], 49152\n\t" \
  "s_nop 0\n\t" \
  "global_load_lds_dwordx4 %[vx0], s[44:45]\n\t" \
  "s_waitcnt lgkmcnt(6)\n\t" \
  "v_mfma_f32_32x32x16_bf16 v[0:15], v[48:51], v[140:143], v[0:15]\n\t" \
  "ds_read_b128 v[152:155], v81 offset:16384\n\t" \
  "ds_read_b64_tr_b16 v[48:49], %[wq0] offset:18432\n\t" \
  "ds_read_b64_tr_b16 v[50:51], %[wq0] offset:19200\n\t" \
  "s_add_u32 m0, %[sx], 50176\n\t" \
  "s_nop 0\n\t" \
  "global_load_lds_dwordx4 %[vx1], s[44:45]\n\t" \
  "s_add_u32 s52, s52, 1\n\t" \
  "s_and_b32 s52, s52, 7\n\t" \
  "s_waitcnt lgkmcnt(6)\n\t" \
  "v_mfma_f32_32x32x16_bf16 v[0:15], v[52:55], v[144:147], v[0:15]\n\t" \
  "ds_read_b128 v[156:159], v82 offset:16384\n\t" \
  "ds_read_b64_tr_b16 v[52:53], %[wq0] offset:21504\n\t" \
  "ds_read_b64_tr_b16 v[54:55], %[wq0] offset:22272\n\t" \
  "s_mul_i32 s44, s53, 196608\n\t" \
  "s_add_u32 s44, s42, s44\n\t" \
  "s_addc_u32 s45, s43, 0\n\t" \
  "s_add_u32 m0, %[sq], 102400\n\t" \
  "s_nop 0\n\t" \
  "global_load_lds_dwordx4 %[vq0], s[44:45]\n\t" \
  "s_waitcnt lgkmcnt(6)\n\t" \
  "v_mfma_f32_32x32x16_bf16 v[0:15], v[56:59], v[148:151], v[0:15]\n\t" \
  "s_waitcnt vmcnt(3)\n\t" \
  "s_barrier\n\t" \
  "ds_read_b128 v[160:163], %[xa] offset:32768\n\t" \
  "ds_read_b64_tr_b16 v[56:57], %[wq0] offset:24576\n\t" \
  "ds_read_b64_tr_b16 v[58:59], %[wq0] offset:25344\n\t" \
  "s_add_u32 m0, %[sq], 103424\n\t" \
  "s_nop 0\n\t" \
  "global_load_lds_dwordx4 %[vq1], s[44:45]\n\t" \
  "s_add_u32 s53, s53, 1\n\t" \
  "s_and_b32 s53, s53, 7\n\t" \
  "s_waitcnt lgkmcnt(6)\n\t" \
  "v_mfma_f32_32x32x16_bf16 v[0:15], v[48:51], v[152:155], v[0:15]\n\t" \
  "ds_read_b128 v[164:167], v80 offset:32768\n\t" \
  "ds_read_b64_tr_b16 v[48:49], %[wq0] offset:27648\n\t" \
  "ds_read_b64_tr_b16 v[50:51], %[wq0] offset:28416\n\t" \
  "s_mul_i32 s44, s53, 196608\n\t" \
  "s_add_u32 s44, s42, s44\n\t" \
  "s_addc_u32 s45, s43, 0\n\t" \
  "s_add_u32 m0, %[sq], 65536\n\t" \
  "s_nop 0\n\t" \
  "global_load_lds_dwordx4 %[vq0], s[44:45]\n\t" \
  "s_waitcnt lgkmcnt(6)\n\t" \
  "v_mfma_f32_32x32x16_bf16 v[0:15], v[52:55], v[156:159], v[0:15]\n\t" \
  "ds_read_b128 v[168:171], v81 offset:32768\n\t" \
  "ds_read_b64_tr_b16 v[52:53], %[wq0] offset:30720\n\t" \
  "ds_read_b64_tr_b16 v[54:55], %[wq0] offset:31488\n\t" \
  "s_add_u32 m0, %[sq], 66560\n\t" \
  "s_nop 0\n\t" \
  "global_load_lds_dwordx4 %[vq1], s[44:45]\n\t" \
  "s_add_u32 s53, s53, 1\n\t" \
  "s_and_b32 s53, s53, 7\n\t" \
  "s_waitcnt lgkmcnt(6)\n\t" \
  "v_mfma_f32_32x32x16_bf16 v[0:15], v[56:59], v[160:163], v[0:15]\n\t" \
  "ds_read_b128 v[172:175], v82 offset:32768\n\t" \
  "ds_read_b64_tr_b16 v[56:57], %[wq0] offset:33792\n\t" \
  "ds_read_b64_tr_b16 v[58:59], %[wq0] offset:34560\n\t" \
  "s_mul_i32 s44, s52, 128\n\t" \
  "s_add_u32 s44, s40, s44\n\t" \
  "s_addc_u32 s45, s41, 0\n\t" \
  "s_add_u32 m0, %[sx], 0\n\t" \
  "s_nop 0\n\t" \
  "global_load_lds_dwordx4 %[vx0], s[44:45]\n\t" \
  "s_waitcnt lgkmcnt(6)\n\t" \
  "v_mfma_f32_32x32x16_bf16 v[0:15], v[48:51], v[164:167], v[0:15]\n\t" \
  "s_waitcnt vmcnt(3)\n\t" \
  "s_barrier\n\t" \
  "ds_read_b128 v[176:179], %[xa] offset:49152\n\t" \
  "ds_read_b64_tr_b16 v[48:49], %[wq0] offset:36864\n\t" \
  "ds_read_b64_tr_b16 v[50:51], %[wq0] offset:37632\n\t" \
  "s_add_u32 m0, %[sx], 1024\n\t" \
  "s_nop 0\n\t" \
  "global_load_lds_dwordx4 %[vx1], s[44:45]\n\t" \
  "s_add_u32 s52, s52, 1\n\t" \
  "s_and_b32 s52, s52, 7\n\t" \
  "s_waitcnt lgkmcnt(6)\n\t" \
  "v_mfma_f32_32x32x16_bf16 v[0:15], v[52:55], v[168:171], v[0:15]\n\t" \
  "ds_read_b128 v[180:183], v80 offset:49152\n\t" \
  "ds_read_b64_tr_b16 v[52:53], %[wq0] offset:39936\n\t" \
  "ds_read_b64_tr_b16 v[54:55], %[wq0] offset:40704\n\t" \
  "global_load_dword v84, %[pfo], s[46:47]\n\t" \
  "s_waitcnt lgkmcnt(6)\n\t" \
  "v_mfma_f32_32x32x16_bf16 v[0:15], v[56:59], v[172:175], v[0:15]\n\t" \
  "ds_read_b128 v[184:187], v81 offset:49152\n\t" \
  "ds_read_b64_tr_b16 v[56:57], %[wq0] offset:43008\n\t" \
  "ds_read_b64_tr_b16 v[58:59], %[wq0] offset:43776\n\t" \
  "s_mul_i32 s44, s53, 196608\n\t" \
  "s_add_u32 s44, s42, s44\n\t" \
  "s_addc_u32 s45, s43, 0\n\t" \
  "s_add_u32 m0, %[sq], 77824\n\t" \
  "s_nop 0\n\t" \
  "global_load_lds_dwordx4 %[vq0], s[44:45]\n\t" \
  "s_waitcnt lgkmcnt(6)\n\t" \
  "v_mfma_f32_32x32x16_bf16 v[0:15], v[48:51], v[176:179], v[0:15]\n\t" \
  "ds_read_b128 v[188:191], v82 offset:49152\n\t" \
  "ds_read_b64_tr_b16 v[48:49], %[wq0] offset:46080\n\t" \
  "ds_read_b64_tr_b16 v[50:51], %[wq0] offset:46848\n\t" \
  "s_add_u32 m0, %[sq], 78848\n\t" \
  "s_nop 0\n\t" \
  "global_load_lds_dwordx4 %[vq1], s[44:45]\n\t" \
  "s_add_u32 s53, s53, 1\n\t" \
  "s_and_b32 s53, s53, 7\n\t" \
  "s_waitcnt lgkmcnt(6)\n\t" \
  "v_mfma_f32_32x32x16_bf16 v[0:15], v[52:55], v[180:183], v[0:15]\n\t" \
  "s_waitcnt vmcnt(3)\n\t" \
  "s_barrier\n\t" \
  "ds_read_b128 v[192:195], %[xa] offset:0\n\t" \
  "ds_read_b64_tr_b16 v[52:53], %[wq0] offset:0\n\t" \
  "ds_read_b64_tr_b16 v[54:55], %[wq0] offset:768\n\t" \
  "s_mul_i32 s44, s52, 128\n\t" \
  "s_add_u32 s44, s40, s44\n\t" \
  "s_addc_u32 s45, s41, 0\n\t" \
  "s_add_u32 m0, %[sx], 16384\n\t" \
  "s_nop 0\n\t" \
  "global_load_lds_dwordx4 %[vx0], s[44:45]\n\t" \
  "s_waitcnt lgkmcnt(6)\n\t" \
  "v_mfma_f32_32x32x16_bf16 v[0:15], v[56:59], v[184:187], v[0:15]\n\t" \
  "ds_read_b128 v[196:199], v80 offset:0\n\t" \
  "ds_read_b64_tr_b16 v[56:57], %[wq0] offset:3072\n\t" \
  "ds_read_b64_tr_b16 v[58:59], %[wq0] offset:3840\n\t" \
  "s_add_u32 m0, %[sx], 17408\n\t" \
  "s_nop 0\n\t" \
  "global_load_lds_dwordx4 %[vx1], s[44:45]\n\t" \
  "s_add_u32 s52, s52, 1\n\t" \
  "s_and_b32 s52, s52, 7\n\t" \
  "s_waitcnt lgkmcnt(6)\n\t" \
  "v_mfma_f32_32x32x16_bf16 v[0:15], v[48:51], v[188:191], v[0:15]\n\t" \
  "ds_read_b128 v[200:203], v81 offset:0\n\t" \
  "ds_read_b64_tr_b16 v[48:49], %[wq0] offset:6144\n\t" \
  "ds_read_b64_tr_b16 v[50:51], %[wq0] offset:6912\n\t" \
  "s_mul_i32 s44, s53, 196608\n\t" \
  "s_add_u32 s44, s42, s44\n\t" \
  "s_addc_u32 s45, s43, 0\n\t" \
  "s_add_u32 m0, %[sq], 90112\n\t" \
  "s_nop 0\n\t" \
  "global_load_lds_dwordx4 %[vq0], s[44:45]\n\t" \
  "s_waitcnt lgkmcnt(6)\n\t" \
  "v_mfma_f32_32x32x16_bf16 v[0:15], v[52:55], v[192:195], v[0:15]\n\t" \
  "ds_read_b128 v[204:207], v82 offset:0\n\t" \
  "ds_read_b64_tr_b16 v[52:53], %[wq0] offset:9216\n\t" \
  "ds_read_b64_tr_b16 v[54:55], %[wq0] offset:9984\n\t" \
  "s_add_u32 m0, %[sq], 91136\n\t" \
  "s_nop 0\n\t" \
  "global_load_lds_dwordx4 %[vq1], s[44:45]\n\t" \
  "s_add_u32 s53, s53, 1\n\t" \
  "s_and_b32 s53, s53, 7\n\t" \
  "s_waitcnt lgkmcnt(6)\n\t" \
  "v_mfma_f32_32x32x16_bf16 v[0:15], v[56:59], v[196:199], v[0:15]\n\t" \
  "s_waitcnt vmcnt(2)\n\t" \
  "s_barrier\n\t" \
  "ds_read_b128 v[208:211], %[xa] offset:16384\n\t" \
  "ds_read_b64_tr_b16 v[56:57], %[wq0] offset:12288\n\t" \
  "ds_read_b64_tr_b16 v[58:59], %[wq0] offset:13056\n\t" \
  "s_mul_i32 s44, s52, 128\n\t" \
  "s_add_u32 s44, s40, s44\n\t" \
  "s_addc_u32 s45, s41, 0\n\t" \
  "s_add_u32 m0, %[sx], 32768\n\t" \
  "s_nop 0\n\t" \
  "global_load_lds_dwordx4 %[vx0], s[44:45]\n\t" \
  "s_waitcnt lgkmcnt(6)\n\t" \
  "v_mfma_f32_32x32x16_bf16 v[0:15], v[48:51], v[200:203], v[0:15]\n\t" \
  "ds_read_b128 v[212:215], v80 offset:16384\n\t" \
  "ds_read_b64_tr_b16 v[48:49], %[wq0] offset:15360\n\t" \
  "ds_read_b64_tr_b16 v[50:51], %[wq0] offset:16128\n\t" \
  "s_add_u32 m0, %[sx], 33792\n\t" \
  "s_nop 0\n\t" \
  "global_load_lds_dwordx4 %[vx1], s[44:45]\n\t" \
  "s_add_u32 s52, s52, 1\n\t" \
  "s_and_b32 s52, s52, 7\n\t" \
  "s_waitcnt lgkmcnt(6)\n\t" \
  "v_mfma_f32_32x32x16_bf16 v[0:15], v[52:55], v[204:207], v[0:15]\n\t" \
  "ds_read_b128 v[216:219], v81 offset:16384\n\t" \
  "ds_read_b64_tr_b16 v[52:53], %[wq0] offset:18432\n\t" \
  "ds_read_b64_tr_b16 v[54:55], %[wq0] offset:19200\n\t" \
  "s_mul_i32 s44, s53, 196608\n\t" \
  "s_add_u32 s44, s42, s44\n\t" \
  "s_addc_u32 s45, s43, 0\n\t" \
  "s_add_u32 m0, %[sq], 102400\n\t" \
  "s_nop 0\n\t" \
  "global_load_lds_dwordx4 %[vq0], s[44:45]\n\t" \
  "s_waitcnt lgkmcnt(6)\n\t" \
  "v_mfma_f32_32x32x16_bf16 v[0:15], v[56:59], v[208:211], v[0:15]\n\t" \
  "ds_read_b128 v[220:223], v82 offset:16384\n\t" \
  "ds_read_b64_tr_b16 v[56:57], %[wq0] offset:21504\n\t" \
  "ds_read_b64_tr_b16 v[58:59], %[wq0] offset:22272\n\t" \
  "s_add_u32 m0, %[sq], 103424\n\t" \
  "s_nop 0\n\t" \
  "global_load_lds_dwordx4 %[vq1], s[44:45]\n\t" \
  "s_add_u32 s53, s53, 1\n\t" \
  "s_and_b32 s53, s53, 7\n\t" \
  "s_waitcnt lgkmcnt(6)\n\t" \
  "v_mfma_f32_32x32x16_bf16 v[0:15], v[48:51], v[212:215], v[0:15]\n\t" \
  "s_waitcnt vmcnt(2)\n\t" \
  "s_barrier\n\t" \
  "ds_read_b128 v[224:227], %[xa] offset:32768\n\t" \
  "ds_read_b64_tr_b16 v[48:49], %[wq0] offset:24576\n\t" \
  "ds_read_b64_tr_b16 v[50:51], %[wq0] offset:25344\n\t" \
  "s_mul_i32 s44, s52, 128\n\t" \
  "s_add_u32 s44, s40, s44\n\t" \
  "s_addc_u32 s45, s41, 0\n\t" \
  "s_add_u32 m0, %[sx], 49152\n\t" \
  "s_nop 0\n\t" \
  "global_load_lds_dwordx4 %[vx0], s[44:45]\n\t" \
  "s_waitcnt lgkmcnt(6)\n\t" \
  "v_mfma_f32_32x32x16_bf16 v[0:15], v[52:55], v[216:219], v[0:15]\n\t" \
  "ds_read_b128 v[228:231], v80 offset:32768\n\t" \
  "ds_read_b64_tr_b16 v[52:53], %[wq0] offset:27648\n\t" \
  "ds_read_b64_tr_b16 v[54:55], %[wq0] offset:28416\n\t" \
  "s_add_u32 m0, %[sx], 50176\n\t" \
  "s_nop 0\n\t" \
  "global_load_lds_dwordx4 %[vx1], s[44:45]\n\t" \
  "s_add_u32 s52, s52, 1\n\t" \
  "s_and_b32 s52, s52, 7\n\t" \
  "s_waitcnt lgkmcnt(6)\n\t" \
  "v_mfma_f32_32x32x16_bf16 v[0:15], v[56:59], v[220:223], v[0:15]\n\t" \
  "ds_read_b128 v[232:235], v81 offset:32768\n\t" \
  "ds_read_b64_tr_b16 v[56:57], %[wq0] offset:30720\n\t" \
  "ds_read_b64_tr_b16 v[58:59], %[wq0] offset:31488\n\t" \
  "s_mul_i32 s44, %[fi], 176128\n\t" \
  "s_add_u32 s44, s46, s44\n\t" \
  "s_addc_u32 s45, s47, 0\n\t" \
  "s_add_u32 m0, %[sw], 114688\n\t" \
  "s_nop 0\n\t" \
  "global_load_lds_dwordx4 %[vf0], s[44:45]\n\t" \
  "s_waitcnt lgkmcnt(6)\n\t" \
  "v_mfma_f32_32x32x16_bf16 v[0:15], v[48:51], v[224:227], v[0:15]\n\t" \
  "ds_read_b128 v[236:239], v82 offset:32768\n\t" \
  "ds_read_b64_tr_b16 v[48:49], %[wq0] offset:33792\n\t" \
  "ds_read_b64_tr_b16 v[50:51], %[wq0] offset:34560\n\t" \
  "s_add_u32 m0, %[sw], 115712\n\t" \
  "s_nop 0\n\t" \
  "global_load_lds_dwordx4 %[vf1], s[44:45]\n\t" \
  "s_waitcnt lgkmcnt(6)\n\t" \
  "v_mfma_f32_32x32x16_bf16 v[0:15], v[52:55], v[228:231], v[0:15]\n\t" \
  "s_waitcnt vmcnt(2)\n\t" \
  "s_barrier\n\t" \
  "ds_read_b128 v[240:243], %[xa] offset:49152\n\t" \
  "ds_read_b64_tr_b16 v[52:53], %[wq0] offset:36864\n\t" \
  "ds_read_b64_tr_b16 v[54:55], %[wq0] offset:37632\n\t" \
  "s_add_u32 m0, %[sw], 116736\n\t" \
  "s_nop 0\n\t" \
  "global_load_lds_dwordx4 %[vf2], s[44:45]\n\t" \
  "s_add_u32 %[fi], %[fi], 1\n\t" \
  "s_and_b32 %[fi], %[fi], 7\n\t" \
  "s_mul_i32 s44, %[fi], 176128\n\t" \
  "s_add_u32 s44, s46, s44\n\t" \
  "s_addc_u32 s45, s47, 0\n\t" \
  "s_add_u32 m0, %[sw], 65536\n\t" \
  "s_nop 0\n\t" \
  "global_load_lds_dwordx4 %[vf0], s[44:45]\n\t" \
  "s_waitcnt lgkmcnt(6)\n\t" \
  "v_mfma_f32_32x32x16_bf16 v[0:15], v[56:59], v[232:235], v[0:15]\n\t" \
  "ds_read_b128 v[244:247], v80 offset:49152\n\t" \
  "ds_read_b64_tr_b16 v[56:57], %[wq0] offset:39936\n\t" \
  "ds_read_b64_tr_b16 v[58:59], %[wq0] offset:40704\n\t" \
  "s_add_u32 m0, %[sw], 66560\n\t" \
  "s_nop 0\n\t" \
  "global_load_lds_dwordx4 %[vf1], s[44:45]\n\t" \
  "s_waitcnt lgkmcnt(6)\n\t" \
  "v_mfma_f32_32x32x16_bf16 v[0:15], v[48:51], v[236:239], v[0:15]\n\t" \
  "ds_read_b128 v[248:251], v81 offset:49152\n\t" \
  "ds_read_b64_tr_b16 v[48:49], %[wq0] offset:43008\n\t" \
  "ds_read_b64_tr_b16 v[50:51], %[wq0] offset:43776\n\t" \
  "s_add_u32 m0, %[sw], 67584\n\t" \
  "s_nop 0\n\t" \
  "global_load_lds_dwordx4 %[vf2], s[44:45]\n\t" \
  "s_add_u32 %[fi], %[fi], 1\n\t" \
  "s_and_b32 %[fi], %[fi], 7\n\t" \
  "s_waitcnt lgkmcnt(6)\n\t" \
  "v_mfma_f32_32x32x16_bf16 v[0:15], v[52:55], v[240:243], v[0:15]\n\t" \
  "ds_read_b128 v[252:255], v82 offset:49152\n\t" \
  "ds_read_b64_tr_b16 v[52:53], %[wq0] offset:46080\n\t" \
  "ds_read_b64_tr_b16 v[54:55], %[wq0] offset:46848\n\t" \
  "s_mul_i32 s44, %[fi], 176128\n\t" \
  "s_add_u32 s44, s46, s44\n\t" \
  "s_addc_u32 s45, s47, 0\n\t" \
  "s_add_u32 m0, %[sw], 0\n\t" \
  "s_nop 0\n\t" \
  "global_load_lds_dwordx4 %[vf0], s[44:45]\n\t" \
  "s_waitcnt lgkmcnt(6)\n\t" \
  "v_mfma_f32_32x32x16_bf16 v[0:15], v[56:59], v[244:247], v[0:15]\n\t" \
  "s_add_u32 m0, %[sw], 1024\n\t" \
  "s_nop 0\n\t" \
  "global_load_lds_dwordx4 %[vf1], s[44:45]\n\t" \
  "s_waitcnt lgkmcnt(3)\n\t" \
  "v_mfma_f32_32x32x16_bf16 v[0:15], v[48:51], v[248:251], v[0:15]\n\t" \
  "s_add_u32 m0, %[sw], 2048\n\t" \
  "s_nop 0\n\t" \
  "global_load_lds_dwordx4 %[vf2], s[44:45]\n\t" \
  "s_add_u32 %[fi], %[fi], 1\n\t" \
  "s_and_b32 %[fi], %[fi], 7\n\t" \
  "s_waitcnt lgkmcnt(0)\n\t" \
  "v_mfma_f32_32x32x16_bf16 v[0:15], v[52:55], v[252:255], v[0:15]\n\t" \
  "s_mov_b32 m0, %[m0s]\n\t" \
  "s_nop 15\n\t" \
  ""
#define P2_ASM \
  "s_mov_b32 %[m0s], m0\n\t" \
  "s_waitcnt vmcnt(8)\n\t" \
  "s_barrier\n\t" \
  "ds_read_b64_tr_b16 v[48:49], %[wfh0] offset:49152\n\t" \
  "ds_read_b64_tr_b16 v[50:51], %[wfh0] offset:49920\n\t" \
  "ds_read_b64_tr_b16 v[52:53], %[wfh1] offset:49152\n\t" \
  "ds_read_b64_tr_b16 v[54:55], %[wfh1] offset:49920\n\t" \
  "ds_read_b64_tr_b16 v[56:57], %[wfh2] offset:49152\n\t" \
  "ds_read_b64_tr_b16 v[58:59], %[wfh2] offset:49920\n\t" \
  "ds_read_b64_tr_b16 v[60:61], %[wfh0] offset:52224\n\t" \
  "ds_read_b64_tr_b16 v[62:63], %[wfh0] offset:52992\n\t" \
  "s_mul_i32 s44, %[fi], 176128\n\t" \
  "s_add_u32 s44, s46, s44\n\t" \
  "s_addc_u32 s45, s47, 0\n\t" \
  "s_add_u32 m0, %[sw], 24576\n\t" \
  "s_nop 0\n\t" \
  "global_load_lds_dwordx4 %[vf0], s[44:45]\n\t" \
  "s_waitcnt lgkmcnt(6)\n\t" \
  "v_mfma_f32_32x32x16_bf16 v[0:15], v[48:51], v[128:131], 0\n\t" \
  "ds_read_b64_tr_b16 v[48:49], %[wfh1] offset:52224\n\t" \
  "ds_read_b64_tr_b16 v[50:51], %[wfh1] offset:52992\n\t" \
  "s_waitcnt lgkmcnt(6)\n\t" \
  "v_mfma_f32_32x32x16_bf16 v[16:31], v[52:55], v[128:131], 0\n\t" \
  "ds_read_b64_tr_b16 v[52:53], %[wfh2] offset:52224\n\t" \
  "ds_read_b64_tr_b16 v[54:55], %[wfh2] offset:52992\n\t" \
  "s_add_u32 m0, %[sw], 25600\n\t" \
  "s_nop 0\n\t" \
  "global_load_lds_dwordx4 %[vf1], s[44:45]\n\t" \
  "s_waitcnt lgkmcnt(6)\n\t" \
  "v_mfma_f32_32x32x16_bf16 v[32:47], v[56:59], v[128:131], 0\n\t" \
  "ds_read_b64_tr_b16 v[56:57], %[wfh0] offset:55296\n\t" \
  "ds_read_b64_tr_b16 v[58:59], %[wfh0] offset:56064\n\t" \
  "s_waitcnt lgkmcnt(6)\n\t" \
  "v_mfma_f32_32x32x16_bf16 v[0:15], v[60:63], v[132:135], v[0:15]\n\t" \
  "ds_read_b64_tr_b16 v[60:61], %[wfh1] offset:55296\n\t" \
  "ds_read_b64_tr_b16 v[62:63], %[wfh1] offset:56064\n\t" \
  "s_add_u32 m0, %[sw], 26624\n\t" \
  "s_nop 0\n\t" \
  "global_load_lds_dwordx4 %[vf2], s[44:45]\n\t" \
  "s_add_u32 %[fi], %[fi], 1\n\t" \
  "s_and_b32 %[fi], %[fi], 7\n\t" \
  "s_waitcnt lgkmcnt(6)\n\t" \
  "v_mfma_f32_32x32x16_bf16 v[16:31], v[48:51], v[132:135], v[16:31]\n\t" \
  "ds_read_b64_tr_b16 v[48:49], %[wfh2] offset:55296\n\t" \
  "ds_read_b64_tr_b16 v[50:51], %[wfh2] offset:56064\n\t" \
  "s_waitcnt lgkmcnt(6)\n\t" \
  "v_mfma_f32_32x32x16_bf16 v[32:47], v[52:55], v[132:135], v[32:47]\n\t" \
  "ds_read_b64_tr_b16 v[52:53], %[wfh0] offset:58368\n\t" \
  "ds_read_b64_tr_b16 v[54:55], %[wfh0] offset:59136\n\t" \
  "s_mul_i32 s44, %[fi], 176128\n\t" \
  "s_add_u32 s44, s46, s44\n\t" \
  "s_addc_u32 s45, s47, 0\n\t" \
  "s_add_u32 m0, %[sw], 90112\n\t" \
  "s_nop 0\n\t" \
  "global_load_lds_dwordx4 %[vf0], s[44:45]\n\t" \
  "s_waitcnt lgkmcnt(6)\n\t" \
  "v_mfma_f32_32x32x16_bf16 v[0:15], v[56:59], v[136:139], v[0:15]\n\t" \
  "ds_read_b64_tr_b16 v[56:57], %[wfh1] offset:58368\n\t" \
  "ds_read_b64_tr_b16 v[58:59], %[wfh1] offset:59136\n\t" \
  "s_waitcnt lgkmcnt(6)\n\t" \
  "v_mfma_f32_32x32x16_bf16 v[16:31], v[60:63], v[136:139], v[16:31]\n\t" \
  "ds_read_b64_tr_b16 v[60:61], %[wfh2] offset:58368\n\t" \
  "ds_read_b64_tr_b16 v[62:63], %[wfh2] offset:59136\n\t" \
  "s_add_u32 m0, %[sw], 91136\n\t" \
  "s_nop 0\n\t" \
  "global_load_lds_dwordx4 %[vf1], s[44:45]\n\t" \
  "s_waitcnt lgkmcnt(6)\n\t" \
  "v_mfma_f32_32x32x16_bf16 v[32:47], v[48:51], v[136:139], v[32:47]\n\t" \
  "s_waitcnt vmcnt(10)\n\t" \
  "s_barrier\n\t" \
  "ds_read_b64_tr_b16 v[48:49], %[wfh0] offset:0\n\t" \
  "ds_read_b64_tr_b16 v[50:51], %[wfh0] offset:768\n\t" \
  "s_waitcnt lgkmcnt(6)\n\t" \
  "v_mfma_f32_32x32x16_bf16 v[0:15], v[52:55], v[140:143], v[0:15]\n\t" \
  "ds_read_b64_tr_b16 v[52:53], %[wfh1] offset:0\n\t" \
  "ds_read_b64_tr_b16 v[54:55], %[wfh1] offset:768\n\t" \
  "s_add_u32 m0, %[sw], 92160\n\t" \
  "s_nop 0\n\t" \
  "global_load_lds_dwordx4 %[vf2], s[44:45]\n\t" \
  "s_add_u32 %[fi], %[fi], 1\n\t" \
  "s_and_b32 %[fi], %[fi], 7\n\t" \
  "s_waitcnt lgkmcnt(6)\n\t" \
  "v_mfma_f32_32x32x16_bf16 v[16:31], v[56:59], v[140:143], v[16:31]\n\t" \
  "ds_read_b64_tr_b16 v[56:57], %[wfh2] offset:0\n\t" \
  "ds_read_b64_tr_b16 v[58:59], %[wfh2] offset:768\n\t" \
  "s_waitcnt lgkmcnt(6)\n\t" \
  "v_mfma_f32_32x32x16_bf16 v[32:47], v[60:63], v[140:143], v[32:47]\n\t" \
  "ds_read_b64_tr_b16 v[60:61], %[wfh0] offset:3072\n\t" \
  "ds_read_b64_tr_b16 v[62:63], %[wfh0] offset:3840\n\t" \
  "s_waitcnt lgkmcnt(6)\n\t" \
  "v_mfma_f32_32x32x16_bf16 v[0:15], v[48:51], v[144:147], v[0:15]\n\t" \
  "ds_read_b64_tr_b16 v[48:49], %[wfh1] offset:3072\n\t" \
  "ds_read_b64_tr_b16 v[50:51], %[wfh1] offset:3840\n\t" \
  "s_waitcnt lgkmcnt(6)\n\t" \
  "v_mfma_f32_32x32x16_bf16 v[16:31], v[52:55], v[144:147], v[16:31]\n\t" \
  "ds_read_b64_tr_b16 v[52:53], %[wfh2] offset:3072\n\t" \
  "ds_read_b64_tr_b16 v[54:55], %[wfh2] offset:3840\n\t" \
  "s_waitcnt lgkmcnt(6)\n\t" \
  "v_mfma_f32_32x32x16_bf16 v[32:47], v[56:59], v[144:147], v[32:47]\n\t" \
  "ds_read_b64_tr_b16 v[56:57], %[wfh0] offset:6144\n\t" \
  "ds_read_b64_tr_b16 v[58:59], %[wfh0] offset:6912\n\t" \
  "s_waitcnt lgkmcnt(6)\n\t" \
  "v_mfma_f32_32x32x16_bf16 v[0:15], v[60:63], v[148:151], v[0:15]\n\t" \
  "ds_read_b64_tr_b16 v[60:61], %[wfh1] offset:6144\n\t" \
  "ds_read_b64_tr_b16 v[62:63], %[wfh1] offset:6912\n\t" \
  "s_waitcnt lgkmcnt(6)\n\t" \
  "v_mfma_f32_32x32x16_bf16 v[16:31], v[48:51], v[148:151], v[16:31]\n\t" \
  "ds_read_b64_tr_b16 v[48:49], %[wfh2] offset:6144\n\t" \
  "ds_read_b64_tr_b16 v[50:51], %[wfh2] offset:6912\n\t" \
  "s_waitcnt lgkmcnt(6)\n\t" \
  "v_mfma_f32_32x32x16_bf16 v[32:47], v[52:55], v[148:151], v[32:47]\n\t" \
  "ds_read_b64_tr_b16 v[52:53], %[wfh0] offset:9216\n\t" \
  "ds_read_b64_tr_b16 v[54:55], %[wfh0] offset:9984\n\t" \
  "s_waitcnt lgkmcnt(6)\n\t" \
  "v_mfma_f32_32x32x16_bf16 v[0:15], v[56:59], v[152:155], v[0:15]\n\t" \
  "ds_read_b64_tr_b16 v[56:57], %[wfh1] offset:9216\n\t" \
  "ds_read_b64_tr_b16 v[58:59], %[wfh1] offset:9984\n\t" \
  "s_waitcnt lgkmcnt(6)\n\t" \
  "v_mfma_f32_32x32x16_bf16 v[16:31], v[60:63], v[152:155], v[16:31]\n\t" \
  "ds_read_b64_tr_b16 v[60:61], %[wfh2] offset:9216\n\t" \
  "ds_read_b64_tr_b16 v[62:63], %[wfh2] offset:9984\n\t" \
  "s_waitcnt lgkmcnt(6)\n\t" \
  "v_mfma_f32_32x32x16_bf16 v[32:47], v[48:51], v[152:155], v[32:47]\n\t" \
  "s_waitcnt vmcnt(6)\n\t" \
  "s_barrier\n\t" \
  "s_cmp_lg_u32 %[sx], 0\n\t" \
  "s_cbranch_scc1 2f\n\t" \
  "s_mov_b64 exec, 1\n\t" \
  "global_store_dwordx2 %[vz], %[tag], %[f1p] sc1\n\t" \
  "s_mov_b64 exec, -1\n\t" \
  "2:\n\t" \
  "ds_read_b64_tr_b16 v[48:49], %[wfl0] offset:0\n\t" \
  "ds_read_b64_tr_b16 v[50:51], %[wfl0] offset:768\n\t" \
  "s_waitcnt lgkmcnt(6)\n\t" \
  "v_mfma_f32_32x32x16_bf16 v[0:15], v[52:55], v[156:159], v[0:15]\n\t" \
  "ds_read_b64_tr_b16 v[52:53], %[wfl1] offset:0\n\t" \
  "ds_read_b64_tr_b16 v[54:55], %[wfl1] offset:768\n\t" \
  "s_mul_i32 s44, %[fi], 176128\n\t" \
  "s_add_u32 s44, s46, s44\n\t" \
  "s_addc_u32 s45, s47, 0\n\t" \
  "s_add_u32 m0, %[sw], 114688\n\t" \
  "s_nop 0\n\t" \
  "global_load_lds_dwordx4 %[vf0], s[44:45]\n\t" \
  "s_waitcnt lgkmcnt(6)\n\t" \
  "v_mfma_f32_32x32x16_bf16 v[16:31], v[56:59], v[156:159], v[16:31]\n\t" \
  "ds_read_b64_tr_b16 v[56:57], %[wfl2] offset:0\n\t" \
  "ds_read_b64_tr_b16 v[58:59], %[wfl2] offset:768\n\t" \
  "s_waitcnt lgkmcnt(6)\n\t" \
  "v_mfma_f32_32x32x16_bf16 v[32:47], v[60:63], v[156:159], v[32:47]\n\t" \
  "ds_read_b64_tr_b16 v[60:61], %[wfl0] offset:3072\n\t" \
  "ds_read_b64_tr_b16 v[62:63], %[wfl0] offset:3840\n\t" \
  "s_add_u32 m0, %[sw], 115712\n\t" \
  "s_nop 0\n\t" \
  "global_load_lds_dwordx4 %[vf1], s[44:45]\n\t" \
  "s_waitcnt lgkmcnt(6)\n\t" \
  "v_mfma_f32_32x32x16_bf16 v[0:15], v[48:51], v[160:163], v[0:15]\n\t" \
  "ds_read_b64_tr_b16 v[48:49], %[wfl1] offset:3072\n\t" \
  "ds_read_b64_tr_b16 v[50:51], %[wfl1] offset:3840\n\t" \
  "s_waitcnt lgkmcnt(6)\n\t" \
  "v_mfma_f32_32x32x16_bf16 v[16:31], v[52:55], v[160:163], v[16:31]\n\t" \
  "ds_read_b64_tr_b16 v[52:53], %[wfl2] offset:3072\n\t" \
  "ds_read_b64_tr_b16 v[54:55], %[wfl2] offset:3840\n\t" \
  "s_add_u32 m0, %[sw], 116736\n\t" \
  "s_nop 0\n\t" \
  "global_load_lds_dwordx4 %[vf2], s[44:45]\n\t" \
  "s_add_u32 %[fi], %[fi], 1\n\t" \
  "s_and_b32 %[fi], %[fi], 7\n\t" \
  "s_waitcnt lgkmcnt(6)\n\t" \
  "v_mfma_f32_32x32x16_bf16 v[32:47], v[56:59], v[160:163], v[32:47]\n\t" \
  "ds_read_b64_tr_b16 v[56:57], %[wfl0] offset:6144\n\t" \
  "ds_read_b64_tr_b16 v[58:59], %[wfl0] offset:6912\n\t" \
  "s_waitcnt lgkmcnt(6)\n\t" \
  "v_mfma_f32_32x32x16_bf16 v[0:15], v[60:63], v[164:167], v[0:15]\n\t" \
  "ds_read_b64_tr_b16 v[60:61], %[wfl1] offset:6144\n\t" \
  "ds_read_b64_tr_b16 v[62:63], %[wfl1] offset:6912\n\t" \
  "s_waitcnt lgkmcnt(6)\n\t" \
  "v_mfma_f32_32x32x16_bf16 v[16:31], v[48:51], v[164:167], v[16:31]\n\t" \
  "ds_read_b64_tr_b16 v[48:49], %[wfl2] offset:6144\n\t" \
  "ds_read_b64_tr_b16 v[50:51], %[wfl2] offset:6912\n\t" \
  "s_waitcnt lgkmcnt(6)\n\t" \
  "v_mfma_f32_32x32x16_bf16 v[32:47], v[52:55], v[164:167], v[32:47]\n\t" \
  "ds_read_b64_tr_b16 v[52:53], %[wfl0] offset:9216\n\t" \
  "ds_read_b64_tr_b16 v[54:55], %[wfl0] offset:9984\n\t" \
  "s_waitcnt lgkmcnt(6)\n\t" \
  "v_mfma_f32_32x32x16_bf16 v[0:15], v[56:59], v[168:171], v[0:15]\n\t" \
  "ds_read_b64_tr_b16 v[56:57], %[wfl1] offset:9216\n\t" \
  "ds_read_b64_tr_b16 v[58:59], %[wfl1] offset:9984\n\t" \
  "s_waitcnt lgkmcnt(6)\n\t" \
  "v_mfma_f32_32x32x16_bf16 v[16:31], v[60:63], v[168:171], v[16:31]\n\t" \
  "ds_read_b64_tr_b16 v[60:61], %[wfl2] offset:9216\n\t" \
  "ds_read_b64_tr_b16 v[62:63], %[wfl2] offset:9984\n\t" \
  "s_waitcnt lgkmcnt(6)\n\t" \
  "v_mfma_f32_32x32x16_bf16 v[32:47], v[48:51], v[168:171], v[32:47]\n\t" \
  "s_waitcnt vmcnt(6)\n\t" \
  "s_barrier\n\t" \
  "ds_read_b64_tr_b16 v[48:49], %[wfl0] offset:24576\n\t" \
  "ds_read_b64_tr_b16 v[50:51], %[wfl0] offset:25344\n\t" \
  "s_waitcnt lgkmcnt(6)\n\t" \
  "v_mfma_f32_32x32x16_bf16 v[0:15], v[52:55], v[172:175], v[0:15]\n\t" \
  "ds_read_b64_tr_b16 v[52:53], %[wfl1] offset:24576\n\t" \
  "ds_read_b64_tr_b16 v[54:55], %[wfl1] offset:25344\n\t" \
  "s_mul_i32 s44, %[fi], 176128\n\t" \
  "s_add_u32 s44, s46, s44\n\t" \
  "s_addc_u32 s45, s47, 0\n\t" \
  "s_add_u32 m0, %[sw], 65536\n\t" \
  "s_nop 0\n\t" \
  "global_load_lds_dwordx4 %[vf0], s[44:45]\n\t" \
  "s_waitcnt lgkmcnt(6)\n\t" \
  "v_mfma_f32_32x32x16_bf16 v[16:31], v[56:59], v[172:175], v[16:31]\n\t" \
  "ds_read_b64_tr_b16 v[56:57], %[wfl2] offset:24576\n\t" \
  "ds_read_b64_tr_b16 v[58:59], %[wfl2] offset:25344\n\t" \
  "s_waitcnt lgkmcnt(6)\n\t" \
  "v_mfma_f32_32x32x16_bf16 v[32:47], v[60:63], v[172:175], v[32:47]\n\t" \
  "ds_read_b64_tr_b16 v[60:61], %[wfl0] offset:27648\n\t" \
  "ds_read_b64_tr_b16 v[62:63], %[wfl0] offset:28416\n\t" \
  "s_add_u32 m0, %[sw], 66560\n\t" \
  "s_nop 0\n\t" \
  "global_load_lds_dwordx4 %[vf1], s[44:45]\n\t" \
  "s_waitcnt lgkmcnt(6)\n\t" \
  "v_mfma_f32_32x32x16_bf16 v[0:15], v[48:51], v[176:179], v[0:15]\n\t" \
  "ds_read_b64_tr_b16 v[48:49], %[wfl1] offset:27648\n\t" \
  "ds_read_b64_tr_b16 v[50:51], %[wfl1] offset:28416\n\t" \
  "s_waitcnt lgkmcnt(6)\n\t" \
  "v_mfma_f32_32x32x16_bf16 v[16:31], v[52:55], v[176:179], v[16:31]\n\t" \
  "ds_read_b64_tr_b16 v[52:53], %[wfl2] offset:27648\n\t" \
  "ds_read_b64_tr_b16 v[54:55], %[wfl2] offset:28416\n\t" \
  "s_add_u32 m0, %[sw], 67584\n\t" \
  "s_nop 0\n\t" \
  "global_load_lds_dwordx4 %[vf2], s[44:45]\n\t" \
  "s_add_u32 %[fi], %[fi], 1\n\t" \
  "s_and_b32 %[fi], %[fi], 7\n\t" \
  "s_waitcnt lgkmcnt(6)\n\t" \
  "v_mfma_f32_32x32x16_bf16 v[32:47], v[56:59], v[176:179], v[32:47]\n\t" \
  "ds_read_b64_tr_b16 v[56:57], %[wfl0] offset:30720\n\t" \
  "ds_read_b64_tr_b16 v[58:59], %[wfl0] offset:31488\n\t" \
  "s_waitcnt lgkmcnt(6)\n\t" \
  "v_mfma_f32_32x32x16_bf16 v[0:15], v[60:63], v[180:183], v[0:15]\n\t" \
  "ds_read_b64_tr_b16 v[60:61], %[wfl1] offset:30720\n\t" \
  "ds_read_b64_tr_b16 v[62:63], %[wfl1] offset:31488\n\t" \
  "s_waitcnt lgkmcnt(6)\n\t" \
  "v_mfma_f32_32x32x16_bf16 v[16:31], v[48:51], v[180:183], v[16:31]\n\t" \
  "ds_read_b64_tr_b16 v[48:49], %[wfl2] offset:30720\n\t" \
  "ds_read_b64_tr_b16 v[50:51], %[wfl2] offset:31488\n\t" \
  "s_waitcnt lgkmcnt(6)\n\t" \
  "v_mfma_f32_32x32x16_bf16 v[32:47], v[52:55], v[180:183], v[32:47]\n\t" \
  "ds_read_b64_tr_b16 v[52:53], %[wfl0] offset:33792\n\t" \
  "ds_read_b64_tr_b16 v[54:55], %[wfl0] offset:34560\n\t" \
  "s_waitcnt lgkmcnt(6)\n\t" \
  "v_mfma_f32_32x32x16_bf16 v[0:15], v[56:59], v[184:187], v[0:15]\n\t" \
  "ds_read_b64_tr_b16 v[56:57], %[wfl1] offset:33792\n\t" \
  "ds_read_b64_tr_b16 v[58:59], %[wfl1] offset:34560\n\t" \
  "s_waitcnt lgkmcnt(6)\n\t" \
  "v_mfma_f32_32x32x16_bf16 v[16:31], v[60:63], v[184:187], v[16:31]\n\t" \
  "ds_read_b64_tr_b16 v[60:61], %[wfl2] offset:33792\n\t" \
  "ds_read_b64_tr_b16 v[62:63], %[wfl2] offset:34560\n\t" \
  "s_waitcnt lgkmcnt(6)\n\t" \
  "v_mfma_f32_32x32x16_bf16 v[32:47], v[48:51], v[184:187], v[32:47]\n\t" \
  "s_waitcnt vmcnt(6)\n\t" \
  "s_barrier\n\t" \
  "ds_read_b64_tr_b16 v[48:49], %[wfh0] offset:24576\n\t" \
  "ds_read_b64_tr_b16 v[50:51], %[wfh0] offset:25344\n\t" \
  "s_waitcnt lgkmcnt(6)\n\t" \
  "v_mfma_f32_32x32x16_bf16 v[0:15], v[52:55], v[188:191], v[0:15]\n\t" \
  "ds_read_b64_tr_b16 v[52:53], %[wfh1] offset:24576\n\t" \
  "ds_read_b64_tr_b16 v[54:55], %[wfh1] offset:25344\n\t" \
  "s_mul_i32 s44, %[fi], 176128\n\t" \
  "s_add_u32 s44, s46, s44\n\t" \
  "s_addc_u32 s45, s47, 0\n\t" \
  "s_add_u32 m0, %[sw], 0\n\t" \
  "s_nop 0\n\t" \
  "global_load_lds_dwordx4 %[vf0], s[44:45]\n\t" \
  "s_waitcnt lgkmcnt(6)\n\t" \
  "v_mfma_f32_32x32x16_bf16 v[16:31], v[56:59], v[188:191], v[16:31]\n\t" \
  "ds_read_b64_tr_b16 v[56:57], %[wfh2] offset:24576\n\t" \
  "ds_read_b64_tr_b16 v[58:59], %[wfh2] offset:25344\n\t" \
  "s_waitcnt lgkmcnt(6)\n\t" \
  "v_mfma_f32_32x32x16_bf16 v[32:47], v[60:63], v[188:191], v[32:47]\n\t" \
  "ds_read_b64_tr_b16 v[60:61], %[wfh0] offset:27648\n\t" \
  "ds_read_b64_tr_b16 v[62:63], %[wfh0] offset:28416\n\t" \
  "s_add_u32 m0, %[sw], 1024\n\t" \
  "s_nop 0\n\t" \
  "global_load_lds_dwordx4 %[vf1], s[44:45]\n\t" \
  "s_waitcnt lgkmcnt(6)\n\t" \
  "v_mfma_f32_32x32x16_bf16 v[0:15], v[48:51], v[192:195], v[0:15]\n\t" \
  "ds_read_b64_tr_b16 v[48:49], %[wfh1] offset:27648\n\t" \
  "ds_read_b64_tr_b16 v[50:51], %[wfh1] offset:28416\n\t" \
  "s_waitcnt lgkmcnt(6)\n\t" \
  "v_mfma_f32_32x32x16_bf16 v[16:31], v[52:55], v[192:195], v[16:31]\n\t" \
  "ds_read_b64_tr_b16 v[52:53], %[wfh2] offset:27648\n\t" \
  "ds_read_b64_tr_b16 v[54:55], %[wfh2] offset:28416\n\t" \
  "s_add_u32 m0, %[sw], 2048\n\t" \
  "s_nop 0\n\t" \
  "global_load_lds_dwordx4 %[vf2], s[44:45]\n\t" \
  "s_add_u32 %[fi], %[fi], 1\n\t" \
  "s_and_b32 %[fi], %[fi], 7\n\t" \
  "s_waitcnt lgkmcnt(6)\n\t" \
  "v_mfma_f32_32x32x16_bf16 v[32:47], v[56:59], v[192:195], v[32:47]\n\t" \
  "ds_read_b64_tr_b16 v[56:57], %[wfh0] offset:30720\n\t" \
  "ds_read_b64_tr_b16 v[58:59], %[wfh0] offset:31488\n\t" \
  "s_waitcnt lgkmcnt(6)\n\t" \
  "v_mfma_f32_32x32x16_bf16 v[0:15], v[60:63], v[196:199], v[0:15]\n\t" \
  "ds_read_b64_tr_b16 v[60:61], %[wfh1] offset:30720\n\t" \
  "ds_read_b64_tr_b16 v[62:63], %[wfh1] offset:31488\n\t" \
  "global_load_dwordx2 v[72:73], %[f1o], %[f1b] sc1\n\t" \
  "s_waitcnt lgkmcnt(6)\n\t" \
  "v_mfma_f32_32x32x16_bf16 v[16:31], v[48:51], v[196:199], v[16:31]\n\t" \
  "ds_read_b64_tr_b16 v[48:49], %[wfh2] offset:30720\n\t" \
  "ds_read_b64_tr_b16 v[50:51], %[wfh2] offset:31488\n\t" \
  "s_waitcnt lgkmcnt(6)\n\t" \
  "v_mfma_f32_32x32x16_bf16 v[32:47], v[52:55], v[196:199], v[32:47]\n\t" \
  "ds_read_b64_tr_b16 v[52:53], %[wfh0] offset:33792\n\t" \
  "ds_read_b64_tr_b16 v[54:55], %[wfh0] offset:34560\n\t" \
  "s_waitcnt lgkmcnt(6)\n\t" \
  "v_mfma_f32_32x32x16_bf16 v[0:15], v[56:59], v[200:203], v[0:15]\n\t" \
  "ds_read_b64_tr_b16 v[56:57], %[wfh1] offset:33792\n\t" \
  "ds_read_b64_tr_b16 v[58:59], %[wfh1] offset:34560\n\t" \
  "s_waitcnt lgkmcnt(6)\n\t" \
  "v_mfma_f32_32x32x16_bf16 v[16:31], v[60:63], v[200:203], v[16:31]\n\t" \
  "ds_read_b64_tr_b16 v[60:61], %[wfh2] offset:33792\n\t" \
  "ds_read_b64_tr_b16 v[62:63], %[wfh2] offset:34560\n\t" \
  "s_waitcnt lgkmcnt(6)\n\t" \
  "v_mfma_f32_32x32x16_bf16 v[32:47], v[48:51], v[200:203], v[32:47]\n\t" \
  "s_waitcnt vmcnt(7)\n\t" \
  "s_barrier\n\t" \
  "ds_read_b64_tr_b16 v[48:49], %[wfh0] offset:49152\n\t" \
  "ds_read_b64_tr_b16 v[50:51], %[wfh0] offset:49920\n\t" \
  "s_waitcnt lgkmcnt(6)\n\t" \
  "v_mfma_f32_32x32x16_bf16 v[0:15], v[52:55], v[204:207], v[0:15]\n\t" \
  "ds_read_b64_tr_b16 v[52:53], %[wfh1] offset:49152\n\t" \
  "ds_read_b64_tr_b16 v[54:55], %[wfh1] offset:49920\n\t" \
  "s_waitcnt lgkmcnt(6)\n\t" \
  "v_mfma_f32_32x32x16_bf16 v[16:31], v[56:59], v[204:207], v[16:31]\n\t" \
  "ds_read_b64_tr_b16 v[56:57], %[wfh2] offset:49152\n\t" \
  "ds_read_b64_tr_b16 v[58:59], %[wfh2] offset:49920\n\t" \
  "s_waitcnt lgkmcnt(6)\n\t" \
  "v_mfma_f32_32x32x16_bf16 v[32:47], v[60:63], v[204:207], v[32:47]\n\t" \
  "ds_read_b64_tr_b16 v[60:61], %[wfh0] offset:52224\n\t" \
  "ds_read_b64_tr_b16 v[62:63], %[wfh0] offset:52992\n\t" \
  "s_waitcnt lgkmcnt(6)\n\t" \
  "v_mfma_f32_32x32x16_bf16 v[0:15], v[48:51], v[208:211], v[0:15]\n\t" \
  "ds_read_b64_tr_b16 v[48:49], %[wfh1] offset:52224\n\t" \
  "ds_read_b64_tr_b16 v[50:51], %[wfh1] offset:52992\n\t" \
  "s_waitcnt lgkmcnt(6)\n\t" \
  "v_mfma_f32_32x32x16_bf16 v[16:31], v[52:55], v[208:211], v[16:31]\n\t" \
  "ds_read_b64_tr_b16 v[52:53], %[wfh2] offset:52224\n\t" \
  "ds_read_b64_tr_b16 v[54:55], %[wfh2] offset:52992\n\t" \
  "s_waitcnt lgkmcnt(6)\n\t" \
  "v_mfma_f32_32x32x16_bf16 v[32:47], v[56:59], v[208:211], v[32:47]\n\t" \
  "ds_read_b64_tr_b16 v[56:57], %[wfh0] offset:55296\n\t" \
  "ds_read_b64_tr_b16 v[58:59], %[wfh0] offset:56064\n\t" \
  "s_waitcnt lgkmcnt(6)\n\t" \
  "v_mfma_f32_32x32x16_bf16 v[0:15], v[60:63], v[212:215], v[0:15]\n\t" \
  "ds_read_b64_tr_b16 v[60:61], %[wfh1] offset:55296\n\t" \
  "ds_read_b64_tr_b16 v[62:63], %[wfh1] offset:56064\n\t" \
  "s_waitcnt lgkmcnt(6)\n\t" \
  "v_mfma_f32_32x32x16_bf16 v[16:31], v[48:51], v[212:215], v[16:31]\n\t" \
  "ds_read_b64_tr_b16 v[48:49], %[wfh2] offset:55296\n\t" \
  "ds_read_b64_tr_b16 v[50:51], %[wfh2] offset:56064\n\t" \
  "s_waitcnt lgkmcnt(6)\n\t" \
  "v_mfma_f32_32x32x16_bf16 v[32:47], v[52:55], v[212:215], v[32:47]\n\t" \
  "ds_read_b64_tr_b16 v[52:53], %[wfh0] offset:58368\n\t" \
  "ds_read_b64_tr_b16 v[54:55], %[wfh0] offset:59136\n\t" \
  "s_waitcnt lgkmcnt(6)\n\t" \
  "v_mfma_f32_32x32x16_bf16 v[0:15], v[56:59], v[216:219], v[0:15]\n\t" \
  "ds_read_b64_tr_b16 v[56:57], %[wfh1] offset:58368\n\t" \
  "ds_read_b64_tr_b16 v[58:59], %[wfh1] offset:59136\n\t" \
  "s_waitcnt lgkmcnt(6)\n\t" \
  "v_mfma_f32_32x32x16_bf16 v[16:31], v[60:63], v[216:219], v[16:31]\n\t" \
  "ds_read_b64_tr_b16 v[60:61], %[wfh2] offset:58368\n\t" \
  "ds_read_b64_tr_b16 v[62:63], %[wfh2] offset:59136\n\t" \
  "s_waitcnt lgkmcnt(6)\n\t" \
  "v_mfma_f32_32x32x16_bf16 v[32:47], v[48:51], v[216:219], v[32:47]\n\t" \
  "s_waitcnt vmcnt(0)\n\t" \
  "s_barrier\n\t" \
  "v_cmp_ne_u64 vcc, v[72:73], %[tag]\n\t" \
  "s_nop 4\n\t" \
  "s_cmp_eq_u64 vcc, 0\n\t" \
  "s_cselect_b32 %[kvok], 1, 0\n\t" \
  "ds_read_b64_tr_b16 v[48:49], %[wfh0] offset:0\n\t" \
  "ds_read_b64_tr_b16 v[50:51], %[wfh0] offset:768\n\t" \
  "s_cmp_eq_u32 %[kvok], 0\n\t" \
  "s_cbranch_scc1 10f\n\t" \
  "s_add_u32 s44, %[t0], 0\n\t" \
  "s_and_b32 s44, s44, 63\n\t" \
  "s_mul_i32 s44, s44, 3072\n\t" \
  "s_add_u32 s44, s48, s44\n\t" \
  "s_addc_u32 s45, s49, 0\n\t" \
  "s_add_u32 m0, %[skv], 0\n\t" \
  "s_nop 0\n\t" \
  "global_load_lds_dwordx4 %[vl16], s[44:45] sc1\n\t" \
  "10:\n\t" \
  "s_waitcnt lgkmcnt(6)\n\t" \
  "v_mfma_f32_32x32x16_bf16 v[0:15], v[52:55], v[220:223], v[0:15]\n\t" \
  "ds_read_b64_tr_b16 v[52:53], %[wfh1] offset:0\n\t" \
  "ds_read_b64_tr_b16 v[54:55], %[wfh1] offset:768\n\t" \
  "s_cmp_eq_u32 %[kvok], 0\n\t" \
  "s_cbranch_scc1 11f\n\t" \
  "s_add_u32 s44, %[t0], 4\n\t" \
  "s_and_b32 s44, s44, 63\n\t" \
  "s_mul_i32 s44, s44, 3072\n\t" \
  "s_add_u32 s44, s48, s44\n\t" \
  "s_addc_u32 s45, s49, 0\n\t" \
  "s_add_u32 m0, %[skv], 8192\n\t" \
  "s_nop 0\n\t" \
  "global_load_lds_dwordx4 %[vl16], s[44:45] sc1\n\t" \
  "11:\n\t" \
  "s_waitcnt lgkmcnt(6)\n\t" \
  "v_mfma_f32_32x32x16_bf16 v[16:31], v[56:59], v[220:223], v[16:31]\n\t" \
  "ds_read_b64_tr_b16 v[56:57], %[wfh2] offset:0\n\t" \
  "ds_read_b64_tr_b16 v[58:59], %[wfh2] offset:768\n\t" \
  "s_cmp_eq_u32 %[kvok], 0\n\t" \
  "s_cbranch_scc1 12f\n\t" \
  "s_add_u32 s44, %[t0], 8\n\t" \
  "s_and_b32 s44, s44, 63\n\t" \
  "s_mul_i32 s44, s44, 3072\n\t" \
  "s_add_u32 s44, s48, s44\n\t" \
  "s_addc_u32 s45, s49, 0\n\t" \
  "s_add_u32 m0, %[skv], 16384\n\t" \
  "s_nop 0\n\t" \
  "global_load_lds_dwordx4 %[vl16], s[44:45] sc1\n\t" \
  "12:\n\t" \
  "s_waitcnt lgkmcnt(6)\n\t" \
  "v_mfma_f32_32x32x16_bf16 v[32:47], v[60:63], v[220:223], v[32:47]\n\t" \
  "ds_read_b64_tr_b16 v[60:61], %[wfh0] offset:3072\n\t" \
  "ds_read_b64_tr_b16 v[62:63], %[wfh0] offset:3840\n\t" \
  "s_cmp_eq_u32 %[kvok], 0\n\t" \
  "s_cbranch_scc1 13f\n\t" \
  "s_add_u32 s44, %[t0], 12\n\t" \
  "s_and_b32 s44, s44, 63\n\t" \
  "s_mul_i32 s44, s44, 3072\n\t" \
  "s_add_u32 s44, s48, s44\n\t" \
  "s_addc_u32 s45, s49, 0\n\t" \
  "s_add_u32 m0, %[skv], 24576\n\t" \
  "s_nop 0\n\t" \
  "global_load_lds_dwordx4 %[vl16], s[44:45] sc1\n\t" \
  "13:\n\t" \
  "s_waitcnt lgkmcnt(6)\n\t" \
  "v_mfma_f32_32x32x16_bf16 v[0:15], v[48:51], v[224:227], v[0:15]\n\t" \
  "ds_read_b64_tr_b16 v[48:49], %[wfh1] offset:3072\n\t" \
  "ds_read_b64_tr_b16 v[50:51], %[wfh1] offset:3840\n\t" \
  "s_cmp_eq_u32 %[kvok], 0\n\t" \
  "s_cbranch_scc1 14f\n\t" \
  "s_add_u32 s44, %[t0], 16\n\t" \
  "s_and_b32 s44, s44, 63\n\t" \
  "s_mul_i32 s44, s44, 3072\n\t" \
  "s_add_u32 s44, s48, s44\n\t" \
  "s_addc_u32 s45, s49, 0\n\t" \
  "s_add_u32 m0, %[skv], 32768\n\t" \
  "s_nop 0\n\t" \
  "global_load_lds_dwordx4 %[vl16], s[44:45] sc1\n\t" \
  "14:\n\t" \
  "s_waitcnt lgkmcnt(6)\n\t" \
  "v_mfma_f32_32x32x16_bf16 v[16:31], v[52:55], v[224:227], v[16:31]\n\t" \
  "ds_read_b64_tr_b16 v[52:53], %[wfh2] offset:3072\n\t" \
  "ds_read_b64_tr_b16 v[54:55], %[wfh2] offset:3840\n\t" \
  "s_cmp_eq_u32 %[kvok], 0\n\t" \
  "s_cbranch_scc1 24f\n\t" \
  "s_add_u32 s44, %[t0], 56\n\t" \
  "s_and_b32 s44, s44, 63\n\t" \
  "s_mul_i32 s44, s44, 3072\n\t" \
  "s_add_u32 s44, s48, s44\n\t" \
  "s_addc_u32 s45, s49, 0\n\t" \
  "s_add_u32 m0, %[skv], 114688\n\t" \
  "s_nop 0\n\t" \
  "global_load_lds_dwordx4 %[vl16], s[44:45] sc1\n\t" \
  "24:\n\t" \
  "s_waitcnt lgkmcnt(6)\n\t" \
  "v_mfma_f32_32x32x16_bf16 v[32:47], v[56:59], v[224:227], v[32:47]\n\t" \
  "ds_read_b64_tr_b16 v[56:57], %[wfh0] offset:6144\n\t" \
  "ds_read_b64_tr_b16 v[58:59], %[wfh0] offset:6912\n\t" \
  "s_cmp_eq_u32 %[kvok], 0\n\t" \
  "s_cbranch_scc1 25f\n\t" \
  "s_add_u32 s44, %[t0], 60\n\t" \
  "s_and_b32 s44, s44, 63\n\t" \
  "s_mul_i32 s44, s44, 3072\n\t" \
  "s_add_u32 s44, s48, s44\n\t" \
  "s_addc_u32 s45, s49, 0\n\t" \
  "s_add_u32 m0, %[skv], 122880\n\t" \
  "s_nop 0\n\t" \
  "global_load_lds_dwordx4 %[vl16], s[44:45] sc1\n\t" \
  "25:\n\t" \
  "s_waitcnt lgkmcnt(6)\n\t" \
  "v_mfma_f32_32x32x16_bf16 v[0:15], v[60:63], v[228:231], v[0:15]\n\t" \
  "ds_read_b64_tr_b16 v[60:61], %[wfh1] offset:6144\n\t" \
  "ds_read_b64_tr_b16 v[62:63], %[wfh1] offset:6912\n\t" \
  "s_cmp_eq_u32 %[kvok], 0\n\t" \
  "s_cbranch_scc1 18f\n\t" \
  "s_add_u32 s44, %[t0], 32\n\t" \
  "s_and_b32 s44, s44, 63\n\t" \
  "s_mul_i32 s44, s44, 3072\n\t" \
  "s_add_u32 s44, s48, s44\n\t" \
  "s_addc_u32 s45, s49, 0\n\t" \
  "s_add_u32 m0, %[skv], 65536\n\t" \
  "s_nop 0\n\t" \
  "global_load_lds_dwordx4 %[vl16], s[44:45] sc1\n\t" \
  "18:\n\t" \
  "s_waitcnt lgkmcnt(6)\n\t" \
  "v_mfma_f32_32x32x16_bf16 v[16:31], v[48:51], v[228:231], v[16:31]\n\t" \
  "ds_read_b64_tr_b16 v[48:49], %[wfh2] offset:6144\n\t" \
  "ds_read_b64_tr_b16 v[50:51], %[wfh2] offset:6912\n\t" \
  "s_cmp_eq_u32 %[kvok], 0\n\t" \
  "s_cbranch_scc1 19f\n\t" \
  "s_add_u32 s44, %[t0], 36\n\t" \
  "s_and_b32 s44, s44, 63\n\t" \
  "s_mul_i32 s44, s44, 3072\n\t" \
  "s_add_u32 s44, s48, s44\n\t" \
  "s_addc_u32 s45, s49, 0\n\t" \
  "s_add_u32 m0, %[skv], 73728\n\t" \
  "s_nop 0\n\t" \
  "global_load_lds_dwordx4 %[vl16], s[44:45] sc1\n\t" \
  "19:\n\t" \
  "s_waitcnt lgkmcnt(6)\n\t" \
  "v_mfma_f32_32x32x16_bf16 v[32:47], v[52:55], v[228:231], v[32:47]\n\t" \
  "ds_read_b64_tr_b16 v[52:53], %[wfh0] offset:9216\n\t" \
  "ds_read_b64_tr_b16 v[54:55], %[wfh0] offset:9984\n\t" \
  "s_cmp_eq_u32 %[kvok], 0\n\t" \
  "s_cbranch_scc1 20f\n\t" \
  "s_add_u32 s44, %[t0], 40\n\t" \
  "s_and_b32 s44, s44, 63\n\t" \
  "s_mul_i32 s44, s44, 3072\n\t" \
  "s_add_u32 s44, s48, s44\n\t" \
  "s_addc_u32 s45, s49, 0\n\t" \
  "s_add_u32 m0, %[skv], 81920\n\t" \
  "s_nop 0\n\t" \
  "global_load_lds_dwordx4 %[vl16], s[44:45] sc1\n\t" \
  "20:\n\t" \
  "s_waitcnt lgkmcnt(6)\n\t" \
  "v_mfma_f32_32x32x16_bf16 v[0:15], v[56:59], v[232:235], v[0:15]\n\t" \
  "ds_read_b64_tr_b16 v[56:57], %[wfh1] offset:9216\n\t" \
  "ds_read_b64_tr_b16 v[58:59], %[wfh1] offset:9984\n\t" \
  "s_waitcnt lgkmcnt(6)\n\t" \
  "v_mfma_f32_32x32x16_bf16 v[16:31], v[60:63], v[232:235], v[16:31]\n\t" \
  "ds_read_b64_tr_b16 v[60:61], %[wfh2] offset:9216\n\t" \
  "ds_read_b64_tr_b16 v[62:63], %[wfh2] offset:9984\n\t" \
  "s_waitcnt lgkmcnt(6)\n\t" \
  "v_mfma_f32_32x32x16_bf16 v[32:47], v[48:51], v[232:235], v[32:47]\n\t" \
  "s_barrier\n\t" \
  "ds_read_b64_tr_b16 v[48:49], %[wfl0] offset:0\n\t" \
  "ds_read_b64_tr_b16 v[50:51], %[wfl0] offset:768\n\t" \
  "s_cmp_eq_u32 %[kvok], 0\n\t" \
  "s_cbranch_scc1 21f\n\t" \
  "s_add_u32 s44, %[t0], 44\n\t" \
  "s_and_b32 s44, s44, 63\n\t" \
  "s_mul_i32 s44, s44, 3072\n\t" \
  "s_add_u32 s44, s48, s44\n\t" \
  "s_addc_u32 s45, s49, 0\n\t" \
  "s_add_u32 m0, %[skv], 90112\n\t" \
  "s_nop 0\n\t" \
  "global_load_lds_dwordx4 %[vl16], s[44:45] sc1\n\t" \
  "21:\n\t" \
  "s_waitcnt lgkmcnt(6)\n\t" \
  "v_mfma_f32_32x32x16_bf16 v[0:15], v[52:55], v[236:239], v[0:15]\n\t" \
  "ds_read_b64_tr_b16 v[52:53], %[wfl1] offset:0\n\t" \
  "ds_read_b64_tr_b16 v[54:55], %[wfl1] offset:768\n\t" \
  "s_cmp_eq_u32 %[kvok], 0\n\t" \
  "s_cbranch_scc1 22f\n\t" \
  "s_add_u32 s44, %[t0], 48\n\t" \
  "s_and_b32 s44, s44, 63\n\t" \
  "s_mul_i32 s44, s44, 3072\n\t" \
  "s_add_u32 s44, s48, s44\n\t" \
  "s_addc_u32 s45, s49, 0\n\t" \
  "s_add_u32 m0, %[skv], 98304\n\t" \
  "s_nop 0\n\t" \
  "global_load_lds_dwordx4 %[vl16], s[44:45] sc1\n\t" \
  "22:\n\t" \
  "s_waitcnt lgkmcnt(6)\n\t" \
  "v_mfma_f32_32x32x16_bf16 v[16:31], v[56:59], v[236:239], v[16:31]\n\t" \
  "ds_read_b64_tr_b16 v[56:57], %[wfl2] offset:0\n\t" \
  "ds_read_b64_tr_b16 v[58:59], %[wfl2] offset:768\n\t" \
  "s_cmp_eq_u32 %[kvok], 0\n\t" \
  "s_cbranch_scc1 23f\n\t" \
  "s_add_u32 s44, %[t0], 52\n\t" \
  "s_and_b32 s44, s44, 63\n\t" \
  "s_mul_i32 s44, s44, 3072\n\t" \
  "s_add_u32 s44, s48, s44\n\t" \
  "s_addc_u32 s45, s49, 0\n\t" \
  "s_add_u32 m0, %[skv], 106496\n\t" \
  "s_nop 0\n\t" \
  "global_load_lds_dwordx4 %[vl16], s[44:45] sc1\n\t" \
  "23:\n\t" \
  "s_waitcnt lgkmcnt(6)\n\t" \
  "v_mfma_f32_32x32x16_bf16 v[32:47], v[60:63], v[236:239], v[32:47]\n\t" \
  "ds_read_b64_tr_b16 v[60:61], %[wfl0] offset:3072\n\t" \
  "ds_read_b64_tr_b16 v[62:63], %[wfl0] offset:3840\n\t" \
  "s_waitcnt lgkmcnt(6)\n\t" \
  "v_mfma_f32_32x32x16_bf16 v[0:15], v[48:51], v[240:243], v[0:15]\n\t" \
  "ds_read_b64_tr_b16 v[48:49], %[wfl1] offset:3072\n\t" \
  "ds_read_b64_tr_b16 v[50:51], %[wfl1] offset:3840\n\t" \
  "s_waitcnt lgkmcnt(6)\n\t" \
  "v_mfma_f32_32x32x16_bf16 v[16:31], v[52:55], v[240:243], v[16:31]\n\t" \
  "ds_read_b64_tr_b16 v[52:53], %[wfl2] offset:3072\n\t" \
  "ds_read_b64_tr_b16 v[54:55], %[wfl2] offset:3840\n\t" \
  "s_waitcnt lgkmcnt(6)\n\t" \
  "v_mfma_f32_32x32x16_bf16 v[32:47], v[56:59], v[240:243], v[32:47]\n\t" \
  "ds_read_b64_tr_b16 v[56:57], %[wfl0] offset:6144\n\t" \
  "ds_read_b64_tr_b16 v[58:59], %[wfl0] offset:6912\n\t" \
  "s_waitcnt lgkmcnt(6)\n\t" \
  "v_mfma_f32_32x32x16_bf16 v[0:15], v[60:63], v[244:247], v[0:15]\n\t" \
  "ds_read_b64_tr_b16 v[60:61], %[wfl1] offset:6144\n\t" \
  "ds_read_b64_tr_b16 v[62:63], %[wfl1] offset:6912\n\t" \
  "s_waitcnt lgkmcnt(6)\n\t" \
  "v_mfma_f32_32x32x16_bf16 v[16:31], v[48:51], v[244:247], v[16:31]\n\t" \
  "ds_read_b64_tr_b16 v[48:49], %[wfl2] offset:6144\n\t" \
  "ds_read_b64_tr_b16 v[50:51], %[wfl2] offset:6912\n\t" \
  "s_waitcnt lgkmcnt(6)\n\t" \
  "v_mfma_f32_32x32x16_bf16 v[32:47], v[52:55], v[244:247], v[32:47]\n\t" \
  "ds_read_b64_tr_b16 v[52:53], %[wfl0] offset:9216\n\t" \
  "ds_read_b64_tr_b16 v[54:55], %[wfl0] offset:9984\n\t" \
  "s_waitcnt lgkmcnt(6)\n\t" \
  "v_mfma_f32_32x32x16_bf16 v[0:15], v[56:59], v[248:251], v[0:15]\n\t" \
  "ds_read_b64_tr_b16 v[56:57], %[wfl1] offset:9216\n\t" \
  "ds_read_b64_tr_b16 v[58:59], %[wfl1] offset:9984\n\t" \
  "s_waitcnt lgkmcnt(6)\n\t" \
  "v_mfma_f32_32x32x16_bf16 v[16:31], v[60:63], v[248:251], v[16:31]\n\t" \
  "ds_read_b64_tr_b16 v[60:61], %[wfl2] offset:9216\n\t" \
  "ds_read_b64_tr_b16 v[62:63], %[wfl2] offset:9984\n\t" \
  "s_waitcnt lgkmcnt(6)\n\t" \
  "v_mfma_f32_32x32x16_bf16 v[32:47], v[48:51], v[248:251], v[32:47]\n\t" \
  "s_waitcnt lgkmcnt(4)\n\t" \
  "v_mfma_f32_32x32x16_bf16 v[0:15], v[52:55], v[252:255], v[0:15]\n\t" \
  "s_waitcnt lgkmcnt(2)\n\t" \
  "v_mfma_f32_32x32x16_bf16 v[16:31], v[56:59], v[252:255], v[16:31]\n\t" \
  "s_waitcnt lgkmcnt(0)\n\t" \
  "v_mfma_f32_32x32x16_bf16 v[32:47], v[60:63], v[252:255], v[32:47]\n\t" \
  "s_mov_b32 m0, %[m0s]\n\t" \
  "s_nop 15\n\t" \
  ""
#define P4_ASM \
  "s_mov_b32 %[m0s], m0\n\t" \
  "v_add_u32 v122, 0x10000, %[pa0]\n\t" \
  "v_add_u32 v123, 0x10000, %[pa1]\n\t" \
  "s_add_u32 m0, %[sw], 0\n\t" \
  "s_nop 0\n\t" \
  "global_load_lds_dwordx4 %[vw0], s[40:41]\n\t" \
  "s_add_u32 m0, %[sw], 1024\n\t" \
  "s_nop 0\n\t" \
  "global_load_lds_dwordx4 %[vw1], s[40:41]\n\t" \
  "s_add_u32 s40, s40, 32768\n\t" \
  "s_addc_u32 s41, s41, 0\n\t" \
  "s_add_u32 m0, %[sw], 16384\n\t" \
  "s_nop 0\n\t" \
  "global_load_lds_dwordx4 %[vw0], s[40:41]\n\t" \
  "s_add_u32 m0, %[sw], 17408\n\t" \
  "s_nop 0\n\t" \
  "global_load_lds_dwordx4 %[vw1], s[40:41]\n\t" \
  "s_add_u32 s40, s40, 32768\n\t" \
  "s_addc_u32 s41, s41, 0\n\t" \
  "s_waitcnt vmcnt(2)\n\t" \
  "s_barrier\n\t" \
  "ds_read_b64_tr_b16 v[96:97], %[pa0] offset:0\n\t" \
  "ds_read_b64_tr_b16 v[98:99], %[pa0] offset:512\n\t" \
  "ds_read_b64_tr_b16 v[100:101], %[pa1] offset:0\n\t" \
  "ds_read_b64_tr_b16 v[102:103], %[pa1] offset:512\n\t" \
  "ds_read_b64_tr_b16 v[104:105], %[pa0] offset:2048\n\t" \
  "ds_read_b64_tr_b16 v[106:107], %[pa0] offset:2560\n\t" \
  "ds_read_b64_tr_b16 v[108:109], %[pa1] offset:2048\n\t" \
  "ds_read_b64_tr_b16 v[110:111], %[pa1] offset:2560\n\t" \
  "s_waitcnt vmcnt(0)\n\t" \
  "s_barrier\n\t" \
  "ds_read_b64_tr_b16 v[112:113], %[pa0] offset:16384\n\t" \
  "ds_read_b64_tr_b16 v[114:115], %[pa0] offset:16896\n\t" \
  "ds_read_b64_tr_b16 v[116:117], %[pa1] offset:16384\n\t" \
  "ds_read_b64_tr_b16 v[118:119], %[pa1] offset:16896\n\t" \
  "s_add_u32 m0, %[sw], 32768\n\t" \
  "s_nop 0\n\t" \
  "global_load_lds_dwordx4 %[vw0], s[40:41]\n\t" \
  "s_add_u32 m0, %[sw], 33792\n\t" \
  "s_nop 0\n\t" \
  "global_load_lds_dwordx4 %[vw1], s[40:41]\n\t" \
  "s_add_u32 s40, s40, 32768\n\t" \
  "s_addc_u32 s41, s41, 0\n\t" \
  "s_add_u32 m0, %[sw], 49152\n\t" \
  "s_nop 0\n\t" \
  "global_load_lds_dwordx4 %[vw0], s[40:41]\n\t" \
  "s_waitcnt lgkmcnt(8)\n\t" \
  "v_mfma_f32_32x32x16_bf16 v[0:15], v[96:99], v[132:135], 0\n\t" \
  "v_mfma_f32_32x32x16_bf16 v[16:31], v[100:103], v[132:135], 0\n\t" \
  "ds_read_b64_tr_b16 v[96:97], %[pa0] offset:18432\n\t" \
  "ds_read_b64_tr_b16 v[98:99], %[pa0] offset:18944\n\t" \
  "ds_read_b64_tr_b16 v[100:101], %[pa1] offset:18432\n\t" \
  "ds_read_b64_tr_b16 v[102:103], %[pa1] offset:18944\n\t" \
  "s_add_u32 m0, %[sw], 50176\n\t" \
  "s_nop 0\n\t" \
  "global_load_lds_dwordx4 %[vw1], s[40:41]\n\t" \
  "s_add_u32 s40, s40, 32768\n\t" \
  "s_addc_u32 s41, s41, 0\n\t" \
  "s_add_u32 m0, %[sw], 65536\n\t" \
  "s_nop 0\n\t" \
  "global_load_lds_dwordx4 %[vw0], s[40:41]\n\t" \
  "s_add_u32 m0, %[sw], 66560\n\t" \
  "s_nop 0\n\t" \
  "global_load_lds_dwordx4 %[vw1], s[40:41]\n\t" \
  "s_add_u32 s40, s40, 32768\n\t" \
  "s_addc_u32 s41, s41, 0\n\t" \
  "s_waitcnt vmcnt(10)\n\t" \
  "s_mov_b64 exec, 1\n\t" \
  "global_store_dwordx2 %[vz], %[tag], %[fmy] sc1\n\t" \
  "s_mov_b64 exec, -1\n\t" \
  "s_waitcnt lgkmcnt(8)\n\t" \
  "v_mfma_f32_32x32x16_bf16 v[0:15], v[104:107], v[136:139], v[0:15]\n\t" \
  "v_mfma_f32_32x32x16_bf16 v[16:31], v[108:111], v[136:139], v[16:31]\n\t" \
  "s_waitcnt vmcnt(5)\n\t" \
  "s_barrier\n\t" \
  "ds_read_b64_tr_b16 v[104:105], %[pa0] offset:32768\n\t" \
  "ds_read_b64_tr_b16 v[106:107], %[pa0] offset:33280\n\t" \
  "ds_read_b64_tr_b16 v[108:109], %[pa1] offset:32768\n\t" \
  "ds_read_b64_tr_b16 v[110:111], %[pa1] offset:33280\n\t" \
  "s_add_u32 m0, %[sw], 81920\n\t" \
  "s_nop 0\n\t" \
  "global_load_lds_dwordx4 %[vw0], s[40:41]\n\t" \
  "s_add_u32 m0, %[sw], 82944\n\t" \
  "s_nop 0\n\t" \
  "global_load_lds_dwordx4 %[vw1], s[40:41]\n\t" \
  "s_add_u32 s40, s40, 32768\n\t" \
  "s_addc_u32 s41, s41, 0\n\t" \
  "s_waitcnt lgkmcnt(8)\n\t" \
  "v_mfma_f32_32x32x16_bf16 v[0:15], v[112:115], v[140:143], v[0:15]\n\t" \
  "v_mfma_f32_32x32x16_bf16 v[16:31], v[116:119], v[140:143], v[16:31]\n\t" \
  "ds_read_b64_tr_b16 v[112:113], %[pa0] offset:34816\n\t" \
  "ds_read_b64_tr_b16 v[114:115], %[pa0] offset:35328\n\t" \
  "ds_read_b64_tr_b16 v[116:117], %[pa1] offset:34816\n\t" \
  "ds_read_b64_tr_b16 v[118:119], %[pa1] offset:35328\n\t" \
  "s_add_u32 m0, %[sw], 98304\n\t" \
  "s_nop 0\n\t" \
  "global_load_lds_dwordx4 %[vw0], s[40:41]\n\t" \
  "s_add_u32 m0, %[sw], 99328\n\t" \
  "s_nop 0\n\t" \
  "global_load_lds_dwordx4 %[vw1], s[40:41]\n\t" \
  "s_add_u32 s40, s40, 32768\n\t" \
  "s_addc_u32 s41, s41, 0\n\t" \
  "s_waitcnt lgkmcnt(8)\n\t" \
  "v_mfma_f32_32x32x16_bf16 v[0:15], v[96:99], v[144:147], v[0:15]\n\t" \
  "v_mfma_f32_32x32x16_bf16 v[16:31], v[100:103], v[144:147], v[16:31]\n\t" \
  "s_waitcnt vmcnt(7)\n\t" \
  "s_barrier\n\t" \
  "ds_read_b64_tr_b16 v[96:97], %[pa0] offset:49152\n\t" \
  "ds_read_b64_tr_b16 v[98:99], %[pa0] offset:49664\n\t" \
  "ds_read_b64_tr_b16 v[100:101], %[pa1] offset:49152\n\t" \
  "ds_read_b64_tr_b16 v[102:103], %[pa1] offset:49664\n\t" \
  "s_add_u32 m0, %[sw], 114688\n\t" \
  "s_nop 0\n\t" \
  "global_load_lds_dwordx4 %[vw0], s[40:41]\n\t" \
  "s_add_u32 m0, %[sw], 115712\n\t" \
  "s_nop 0\n\t" \
  "global_load_lds_dwordx4 %[vw1], s[40:41]\n\t" \
  "s_add_u32 s40, s40, 32768\n\t" \
  "s_addc_u32 s41, s41, 0\n\t" \
  "s_waitcnt lgkmcnt(8)\n\t" \
  "v_mfma_f32_32x32x16_bf16 v[0:15], v[104:107], v[148:151], v[0:15]\n\t" \
  "v_mfma_f32_32x32x16_bf16 v[16:31], v[108:111], v[148:151], v[16:31]\n\t" \
  "ds_read_b64_tr_b16 v[104:105], %[pa0] offset:51200\n\t" \
  "ds_read_b64_tr_b16 v[106:107], %[pa0] offset:51712\n\t" \
  "ds_read_b64_tr_b16 v[108:109], %[pa1] offset:51200\n\t" \
  "ds_read_b64_tr_b16 v[110:111], %[pa1] offset:51712\n\t" \
  "s_add_u32 m0, %[sw], 0\n\t" \
  "s_nop 0\n\t" \
  "global_load_lds_dwordx4 %[vw0], s[40:41]\n\t" \
  "s_waitcnt lgkmcnt(8)\n\t" \
  "v_mfma_f32_32x32x16_bf16 v[0:15], v[112:115], v[152:155], v[0:15]\n\t" \
  "v_mfma_f32_32x32x16_bf16 v[16:31], v[116:119], v[152:155], v[16:31]\n\t" \
  "s_waitcnt vmcnt(8)\n\t" \
  "s_barrier\n\t" \
  "ds_read_b64_tr_b16 v[112:113], v122 offset:0\n\t" \
  "ds_read_b64_tr_b16 v[114:115], v122 offset:512\n\t" \
  "ds_read_b64_tr_b16 v[116:117], v123 offset:0\n\t" \
  "ds_read_b64_tr_b16 v[118:119], v123 offset:512\n\t" \
  "s_add_u32 m0, %[sw], 1024\n\t" \
  "s_nop 0\n\t" \
  "global_load_lds_dwordx4 %[vw1], s[40:41]\n\t" \
  "s_add_u32 s40, s40, 32768\n\t" \
  "s_addc_u32 s41, s41, 0\n\t" \
  "s_add_u32 m0, %[sw], 16384\n\t" \
  "s_nop 0\n\t" \
  "global_load_lds_dwordx4 %[vw0], s[40:41]\n\t" \
  "s_waitcnt lgkmcnt(8)\n\t" \
  "v_mfma_f32_32x32x16_bf16 v[0:15], v[96:99], v[156:159], v[0:15]\n\t" \
  "v_mfma_f32_32x32x16_bf16 v[16:31], v[100:103], v[156:159], v[16:31]\n\t" \
  "ds_read_b64_tr_b16 v[96:97], v122 offset:2048\n\t" \
  "ds_read_b64_tr_b16 v[98:99], v122 offset:2560\n\t" \
  "ds_read_b64_tr_b16 v[100:101], v123 offset:2048\n\t" \
  "ds_read_b64_tr_b16 v[102:103], v123 offset:2560\n\t" \
  "s_add_u32 m0, %[sw], 17408\n\t" \
  "s_nop 0\n\t" \
  "global_load_lds_dwordx4 %[vw1], s[40:41]\n\t" \
  "s_add_u32 s40, s40, %[lb]\n\t" \
  "s_addc_u32 s41, s41, 0\n\t" \
  "s_waitcnt lgkmcnt(8)\n\t" \
  "v_mfma_f32_32x32x16_bf16 v[0:15], v[104:107], v[160:163], v[0:15]\n\t" \
  "v_mfma_f32_32x32x16_bf16 v[16:31], v[108:111], v[160:163], v[16:31]\n\t" \
  "s_waitcnt vmcnt(8)\n\t" \
  "s_barrier\n\t" \
  "ds_read_b64_tr_b16 v[104:105], v122 offset:16384\n\t" \
  "ds_read_b64_tr_b16 v[106:107], v122 offset:16896\n\t" \
  "ds_read_b64_tr_b16 v[108:109], v123 offset:16384\n\t" \
  "ds_read_b64_tr_b16 v[110:111], v123 offset:16896\n\t" \
  "s_add_u32 m0, %[sw], 32768\n\t" \
  "s_nop 0\n\t" \
  "global_load_lds_dwordx4 %[vw0], s[40:41]\n\t" \
  "s_waitcnt lgkmcnt(8)\n\t" \
  "v_mfma_f32_32x32x16_bf16 v[0:15], v[112:115], v[164:167], v[0:15]\n\t" \
  "v_mfma_f32_32x32x16_bf16 v[16:31], v[116:119], v[164:167], v[16:31]\n\t" \
  "ds_read_b64_tr_b16 v[112:113], v122 offset:18432\n\t" \
  "ds_read_b64_tr_b16 v[114:115], v122 offset:18944\n\t" \
  "ds_read_b64_tr_b16 v[116:117], v123 offset:18432\n\t" \
  "ds_read_b64_tr_b16 v[118:119], v123 offset:18944\n\t" \
  "s_add_u32 m0, %[sw], 33792\n\t" \
  "s_nop 0\n\t" \
  "global_load_lds_dwordx4 %[vw1], s[40:41]\n\t" \
  "s_add_u32 s40, s40, 32768\n\t" \
  "s_addc_u32 s41, s41, 0\n\t" \
  "s_waitcnt lgkmcnt(8)\n\t" \
  "v_mfma_f32_32x32x16_bf16 v[0:15], v[96:99], v[168:171], v[0:15]\n\t" \
  "v_mfma_f32_32x32x16_bf16 v[16:31], v[100:103], v[168:171], v[16:31]\n\t" \
  "s_waitcnt vmcnt(8)\n\t" \
  "s_barrier\n\t" \
  "ds_read_b64_tr_b16 v[96:97], v122 offset:32768\n\t" \
  "ds_read_b64_tr_b16 v[98:99], v122 offset:33280\n\t" \
  "ds_read_b64_tr_b16 v[100:101], v123 offset:32768\n\t" \
  "ds_read_b64_tr_b16 v[102:103], v123 offset:33280\n\t" \
  "s_add_u32 m0, %[sw], 49152\n\t" \
  "s_nop 0\n\t" \
  "global_load_lds_dwordx4 %[vw0], s[42:43]\n\t" \
  "s_waitcnt lgkmcnt(8)\n\t" \
  "v_mfma_f32_32x32x16_bf16 v[0:15], v[104:107], v[172:175], v[0:15]\n\t" \
  "v_mfma_f32_32x32x16_bf16 v[16:31], v[108:111], v[172:175], v[16:31]\n\t" \
  "ds_read_b64_tr_b16 v[104:105], v122 offset:34816\n\t" \
  "ds_read_b64_tr_b16 v[106:107], v122 offset:35328\n\t" \
  "ds_read_b64_tr_b16 v[108:109], v123 offset:34816\n\t" \
  "ds_read_b64_tr_b16 v[110:111], v123 offset:35328\n\t" \
  "s_add_u32 m0, %[sw], 50176\n\t" \
  "s_nop 0\n\t" \
  "global_load_lds_dwordx4 %[vw1], s[42:43]\n\t" \
  "s_add_u32 s42, s42, 32768\n\t" \
  "s_addc_u32 s43, s43, 0\n\t" \
  "s_waitcnt lgkmcnt(8)\n\t" \
  "v_mfma_f32_32x32x16_bf16 v[0:15], v[112:115], v[176:179], v[0:15]\n\t" \
  "v_mfma_f32_32x32x16_bf16 v[16:31], v[116:119], v[176:179], v[16:31]\n\t" \
  "s_waitcnt vmcnt(8)\n\t" \
  "s_barrier\n\t" \
  "ds_read_b64_tr_b16 v[112:113], v122 offset:49152\n\t" \
  "ds_read_b64_tr_b16 v[114:115], v122 offset:49664\n\t" \
  "ds_read_b64_tr_b16 v[116:117], v123 offset:49152\n\t" \
  "ds_read_b64_tr_b16 v[118:119], v123 offset:49664\n\t" \
  "s_add_u32 m0, %[sw], 65536\n\t" \
  "s_nop 0\n\t" \
  "global_load_lds_dwordx4 %[vw0], s[42:43]\n\t" \
  "s_waitcnt lgkmcnt(8)\n\t" \
  "v_mfma_f32_32x32x16_bf16 v[0:15], v[96:99], v[180:183], v[0:15]\n\t" \
  "v_mfma_f32_32x32x16_bf16 v[16:31], v[100:103], v[180:183], v[16:31]\n\t" \
  "ds_read_b64_tr_b16 v[96:97], v122 offset:51200\n\t" \
  "ds_read_b64_tr_b16 v[98:99], v122 offset:51712\n\t" \
  "ds_read_b64_tr_b16 v[100:101], v123 offset:51200\n\t" \
  "ds_read_b64_tr_b16 v[102:103], v123 offset:51712\n\t" \
  "s_add_u32 m0, %[sw], 66560\n\t" \
  "s_nop 0\n\t" \
  "global_load_lds_dwordx4 %[vw1], s[42:43]\n\t" \
  "s_add_u32 s42, s42, 32768\n\t" \
  "s_addc_u32 s43, s43, 0\n\t" \
  "s_waitcnt lgkmcnt(8)\n\t" \
  "v_mfma_f32_32x32x16_bf16 v[0:15], v[104:107], v[184:187], v[0:15]\n\t" \
  "v_mfma_f32_32x32x16_bf16 v[16:31], v[108:111], v[184:187], v[16:31]\n\t" \
  "s_waitcnt vmcnt(8)\n\t" \
  "s_barrier\n\t" \
  "ds_read_b64_tr_b16 v[104:105], %[pa0] offset:0\n\t" \
  "ds_read_b64_tr_b16 v[106:107], %[pa0] offset:512\n\t" \
  "ds_read_b64_tr_b16 v[108:109], %[pa1] offset:0\n\t" \
  "ds_read_b64_tr_b16 v[110:111], %[pa1] offset:512\n\t" \
  "global_load_dwordx2 v[120:121], %[f3o], %[f3b] sc1\n\t" \
  "s_add_u32 m0, %[sw], 81920\n\t" \
  "s_nop 0\n\t" \
  "global_load_lds_dwordx4 %[vw0], s[42:43]\n\t" \
  "s_waitcnt lgkmcnt(8)\n\t" \
  "v_mfma_f32_32x32x16_bf16 v[0:15], v[112:115], v[188:191], v[0:15]\n\t" \
  "v_mfma_f32_32x32x16_bf16 v[16:31], v[116:119], v[188:191], v[16:31]\n\t" \
  "ds_read_b64_tr_b16 v[112:113], %[pa0] offset:2048\n\t" \
  "ds_read_b64_tr_b16 v[114:115], %[pa0] offset:2560\n\t" \
  "ds_read_b64_tr_b16 v[116:117], %[pa1] offset:2048\n\t" \
  "ds_read_b64_tr_b16 v[118:119], %[pa1] offset:2560\n\t" \
  "s_add_u32 m0, %[sw], 82944\n\t" \
  "s_nop 0\n\t" \
  "global_load_lds_dwordx4 %[vw1], s[42:43]\n\t" \
  "s_add_u32 s42, s42, 32768\n\t" \
  "s_addc_u32 s43, s43, 0\n\t" \
  "s_waitcnt lgkmcnt(8)\n\t" \
  "v_mfma_f32_32x32x16_bf16 v[0:15], v[96:99], v[192:195], v[0:15]\n\t" \
  "v_mfma_f32_32x32x16_bf16 v[16:31], v[100:103], v[192:195], v[16:31]\n\t" \
  "s_waitcnt vmcnt(9)\n\t" \
  "s_barrier\n\t" \
  "ds_read_b64_tr_b16 v[96:97], %[pa0] offset:16384\n\t" \
  "ds_read_b64_tr_b16 v[98:99], %[pa0] offset:16896\n\t" \
  "ds_read_b64_tr_b16 v[100:101], %[pa1] offset:16384\n\t" \
  "ds_read_b64_tr_b16 v[102:103], %[pa1] offset:16896\n\t" \
  "s_add_u32 m0, %[sw], 98304\n\t" \
  "s_nop 0\n\t" \
  "global_load_lds_dwordx4 %[vw0], s[42:43]\n\t" \
  "s_waitcnt lgkmcnt(8)\n\t" \
  "v_mfma_f32_32x32x16_bf16 v[0:15], v[104:107], v[196:199], v[0:15]\n\t" \
  "v_mfma_f32_32x32x16_bf16 v[16:31], v[108:111], v[196:199], v[16:31]\n\t" \
  "ds_read_b64_tr_b16 v[104:105], %[pa0] offset:18432\n\t" \
  "ds_read_b64_tr_b16 v[106:107], %[pa0] offset:18944\n\t" \
  "ds_read_b64_tr_b16 v[108:109], %[pa1] offset:18432\n\t" \
  "ds_read_b64_tr_b16 v[110:111], %[pa1] offset:18944\n\t" \
  "s_waitcnt vmcnt(3)\n\t" \
  "s_mov_b32 s46, 0x100000\n\t" \
  "4:\n\t" \
  "v_cmp_ne_u64 vcc, v[120:121], %[tag]\n\t" \
  "s_nop 4\n\t" \
  "s_cmp_eq_u64 vcc, 0\n\t" \
  "s_cbranch_scc1 5f\n\t" \
  "s_sleep 2\n\t" \
  "global_load_dwordx2 v[120:121], %[f3o], %[f3b] sc1\n\t" \
  "s_waitcnt vmcnt(0)\n\t" \
  "s_sub_u32 s46, s46, 1\n\t" \
  "s_cmp_lg_u32 s46, 0\n\t" \
  "s_cbranch_scc1 4b\n\t" \
  "5:\n\t" \
  "global_load_dwordx4 v[32:35], %[vo], s[44:45] sc1\n\t" \
  "s_add_u32 s44, s44, 0x10000\n\t" \
  "s_addc_u32 s45, s45, 0\n\t" \
  "global_load_dwordx4 v[36:39], %[vo], s[44:45] sc1\n\t" \
  "s_add_u32 s44, s44, 0x10000\n\t" \
  "s_addc_u32 s45, s45, 0\n\t" \
  "s_waitcnt lgkmcnt(8)\n\t" \
  "v_mfma_f32_32x32x16_bf16 v[0:15], v[112:115], v[200:203], v[0:15]\n\t" \
  "v_mfma_f32_32x32x16_bf16 v[16:31], v[116:119], v[200:203], v[16:31]\n\t" \
  "s_waitcnt vmcnt(10)\n\t" \
  "s_barrier\n\t" \
  "ds_read_b64_tr_b16 v[112:113], %[pa0] offset:32768\n\t" \
  "ds_read_b64_tr_b16 v[114:115], %[pa0] offset:33280\n\t" \
  "ds_read_b64_tr_b16 v[116:117], %[pa1] offset:32768\n\t" \
  "ds_read_b64_tr_b16 v[118:119], %[pa1] offset:33280\n\t" \
  "global_load_dwordx4 v[40:43], %[vo], s[44:45] sc1\n\t" \
  "s_add_u32 s44, s44, 0x10000\n\t" \
  "s_addc_u32 s45, s45, 0\n\t" \
  "global_load_dwordx4 v[44:47], %[vo], s[44:45] sc1\n\t" \
  "s_add_u32 s44, s44, 0x10000\n\t" \
  "s_addc_u32 s45, s45, 0\n\t" \
  "s_waitcnt lgkmcnt(8)\n\t" \
  "v_mfma_f32_32x32x16_bf16 v[0:15], v[96:99], v[204:207], v[0:15]\n\t" \
  "v_mfma_f32_32x32x16_bf16 v[16:31], v[100:103], v[204:207], v[16:31]\n\t" \
  "ds_read_b64_tr_b16 v[96:97], %[pa0] offset:34816\n\t" \
  "ds_read_b64_tr_b16 v[98:99], %[pa0] offset:35328\n\t" \
  "ds_read_b64_tr_b16 v[100:101], %[pa1] offset:34816\n\t" \
  "ds_read_b64_tr_b16 v[102:103], %[pa1] offset:35328\n\t" \
  "global_load_dwordx4 v[48:51], %[vo], s[44:45] sc1\n\t" \
  "s_add_u32 s44, s44, 0x10000\n\t" \
  "s_addc_u32 s45, s45, 0\n\t" \
  "global_load_dwordx4 v[52:55], %[vo], s[44:45] sc1\n\t" \
  "s_add_u32 s44, s44, 0x10000\n\t" \
  "s_addc_u32 s45, s45, 0\n\t" \
  "s_waitcnt lgkmcnt(8)\n\t" \
  "v_mfma_f32_32x32x16_bf16 v[0:15], v[104:107], v[208:211], v[0:15]\n\t" \
  "v_mfma_f32_32x32x16_bf16 v[16:31], v[108:111], v[208:211], v[16:31]\n\t" \
  "s_waitcnt vmcnt(12)\n\t" \
  "s_barrier\n\t" \
  "ds_read_b64_tr_b16 v[104:105], %[pa0] offset:49152\n\t" \
  "ds_read_b64_tr_b16 v[106:107], %[pa0] offset:49664\n\t" \
  "ds_read_b64_tr_b16 v[108:109], %[pa1] offset:49152\n\t" \
  "ds_read_b64_tr_b16 v[110:111], %[pa1] offset:49664\n\t" \
  "global_load_dwordx4 v[56:59], %[vo], s[44:45] sc1\n\t" \
  "s_add_u32 s44, s44, 0x10000\n\t" \
  "s_addc_u32 s45, s45, 0\n\t" \
  "global_load_dwordx4 v[60:63], %[vo], s[44:45] sc1\n\t" \
  "s_waitcnt lgkmcnt(8)\n\t" \
  "v_mfma_f32_32x32x16_bf16 v[0:15], v[112:115], v[212:215], v[0:15]\n\t" \
  "v_mfma_f32_32x32x16_bf16 v[16:31], v[116:119], v[212:215], v[16:31]\n\t" \
  "ds_read_b64_tr_b16 v[112:113], %[pa0] offset:51200\n\t" \
  "ds_read_b64_tr_b16 v[114:115], %[pa0] offset:51712\n\t" \
  "ds_read_b64_tr_b16 v[116:117], %[pa1] offset:51200\n\t" \
  "ds_read_b64_tr_b16 v[118:119], %[pa1] offset:51712\n\t" \
  "s_add_u32 m0, %[sw], 99328\n\t" \
  "s_nop 0\n\t" \
  "global_load_lds_dwordx4 %[vw1], s[42:43]\n\t" \
  "s_add_u32 s42, s42, 32768\n\t" \
  "s_addc_u32 s43, s43, 0\n\t" \
  "s_waitcnt lgkmcnt(8)\n\t" \
  "s_cmp_lt_u32 %[pkq], 2\n\t" \
  "s_cbranch_scc0 6f\n\t" \
  "v_mfma_f32_32x32x16_bf16 v[0:15], v[96:99], v[216:219], v[0:15]\n\t" \
  "v_mfma_f32_32x32x16_bf16 v[16:31], v[100:103], v[216:219], v[16:31]\n\t" \
  "6:\n\t" \
  "s_waitcnt vmcnt(13)\n\t" \
  "s_barrier\n\t" \
  "ds_read_b64_tr_b16 v[96:97], v122 offset:0\n\t" \
  "ds_read_b64_tr_b16 v[98:99], v122 offset:512\n\t" \
  "ds_read_b64_tr_b16 v[100:101], v123 offset:0\n\t" \
  "ds_read_b64_tr_b16 v[102:103], v123 offset:512\n\t" \
  "s_waitcnt vmcnt(1)\n\t" \
  "s_waitcnt lgkmcnt(8)\n\t" \
  "v_mfma_f32_32x32x16_bf16 v[64:79], v[104:107], v[32:35], 0\n\t" \
  "v_mfma_f32_32x32x16_bf16 v[80:95], v[108:111], v[32:35], 0\n\t" \
  "ds_read_b64_tr_b16 v[104:105], v122 offset:2048\n\t" \
  "ds_read_b64_tr_b16 v[106:107], v122 offset:2560\n\t" \
  "ds_read_b64_tr_b16 v[108:109], v123 offset:2048\n\t" \
  "ds_read_b64_tr_b16 v[110:111], v123 offset:2560\n\t" \
  "s_waitcnt lgkmcnt(8)\n\t" \
  "v_mfma_f32_32x32x16_bf16 v[64:79], v[112:115], v[36:39], v[64:79]\n\t" \
  "v_mfma_f32_32x32x16_bf16 v[80:95], v[116:119], v[36:39], v[80:95]\n\t" \
  "s_waitcnt vmcnt(10)\n\t" \
  "s_barrier\n\t" \
  "ds_read_b64_tr_b16 v[112:113], v122 offset:16384\n\t" \
  "ds_read_b64_tr_b16 v[114:115], v122 offset:16896\n\t" \
  "ds_read_b64_tr_b16 v[116:117], v123 offset:16384\n\t" \
  "ds_read_b64_tr_b16 v[118:119], v123 offset:16896\n\t" \
  "s_waitcnt lgkmcnt(8)\n\t" \
  "v_mfma_f32_32x32x16_bf16 v[64:79], v[96:99], v[40:43], v[64:79]\n\t" \
  "v_mfma_f32_32x32x16_bf16 v[80:95], v[100:103], v[40:43], v[80:95]\n\t" \
  "ds_read_b64_tr_b16 v[96:97], v122 offset:18432\n\t" \
  "ds_read_b64_tr_b16 v[98:99], v122 offset:18944\n\t" \
  "ds_read_b64_tr_b16 v[100:101], v123 offset:18432\n\t" \
  "ds_read_b64_tr_b16 v[102:103], v123 offset:18944\n\t" \
  "v_pk_mul_f32 v[0:1], v[0:1], %[g1]\n\t" \
  "v_pk_mul_f32 v[2:3], v[2:3], %[g1]\n\t" \
  "v_pk_mul_f32 v[4:5], v[4:5], %[g1]\n\t" \
  "v_pk_mul_f32 v[6:7], v[6:7], %[g1]\n\t" \
  "s_waitcnt lgkmcnt(8)\n\t" \
  "v_mfma_f32_32x32x16_bf16 v[64:79], v[104:107], v[44:47], v[64:79]\n\t" \
  "v_mfma_f32_32x32x16_bf16 v[80:95], v[108:111], v[44:47], v[80:95]\n\t" \
  "s_waitcnt vmcnt(0)\n\t" \
  "s_barrier\n\t" \
  "ds_read_b64_tr_b16 v[104:105], v122 offset:32768\n\t" \
  "ds_read_b64_tr_b16 v[106:107], v122 offset:33280\n\t" \
  "ds_read_b64_tr_b16 v[108:109], v123 offset:32768\n\t" \
  "ds_read_b64_tr_b16 v[110:111], v123 offset:33280\n\t" \
  "v_pk_mul_f32 v[8:9], v[8:9], %[g1]\n\t" \
  "v_pk_mul_f32 v[10:11], v[10:11], %[g1]\n\t" \
  "v_pk_mul_f32 v[12:13], v[12:13], %[g1]\n\t" \
  "v_pk_mul_f32 v[14:15], v[14:15], %[g1]\n\t" \
  "s_waitcnt lgkmcnt(8)\n\t" \
  "v_mfma_f32_32x32x16_bf16 v[64:79], v[112:115], v[48:51], v[64:79]\n\t" \
  "v_mfma_f32_32x32x16_bf16 v[80:95], v[116:119], v[48:51], v[80:95]\n\t" \
  "ds_read_b64_tr_b16 v[112:113], v122 offset:34816\n\t" \
  "ds_read_b64_tr_b16 v[114:115], v122 offset:35328\n\t" \
  "ds_read_b64_tr_b16 v[116:117], v123 offset:34816\n\t" \
  "ds_read_b64_tr_b16 v[118:119], v123 offset:35328\n\t" \
  "v_pk_mul_f32 v[16:17], v[16:17], %[g1]\n\t" \
  "v_pk_mul_f32 v[18:19], v[18:19], %[g1]\n\t" \
  "v_pk_mul_f32 v[20:21], v[20:21], %[g1]\n\t" \
  "v_pk_mul_f32 v[22:23], v[22:23], %[g1]\n\t" \
  "s_waitcnt lgkmcnt(8)\n\t" \
  "v_mfma_f32_32x32x16_bf16 v[64:79], v[96:99], v[52:55], v[64:79]\n\t" \
  "v_mfma_f32_32x32x16_bf16 v[80:95], v[100:103], v[52:55], v[80:95]\n\t" \
  "v_pk_mul_f32 v[24:25], v[24:25], %[g1]\n\t" \
  "v_pk_mul_f32 v[26:27], v[26:27], %[g1]\n\t" \
  "v_pk_mul_f32 v[28:29], v[28:29], %[g1]\n\t" \
  "v_pk_mul_f32 v[30:31], v[30:31], %[g1]\n\t" \
  "s_waitcnt lgkmcnt(4)\n\t" \
  "v_mfma_f32_32x32x16_bf16 v[64:79], v[104:107], v[56:59], v[64:79]\n\t" \
  "v_mfma_f32_32x32x16_bf16 v[80:95], v[108:111], v[56:59], v[80:95]\n\t" \
  "s_waitcnt lgkmcnt(0)\n\t" \
  "v_mfma_f32_32x32x16_bf16 v[64:79], v[112:115], v[60:63], v[64:79]\n\t" \
  "v_mfma_f32_32x32x16_bf16 v[80:95], v[116:119], v[60:63], v[80:95]\n\t" \
  "s_mov_b32 m0, %[m0s]\n\t" \
  "s_nop 15\n\t" \
  ""

namespace {

typedef float f32x2 __attribute__((ext_vector_type(2)));
typedef float f32x4 __attribute__((ext_vector_type(4)));
typedef float f32x8 __attribute__((ext_vector_type(8)));
typedef float f32x16 __attribute__((ext_vector_type(16)));
typedef unsigned u32x2 __attribute__((ext_vector_type(2)));
typedef unsigned u32x4 __attribute__((ext_vector_type(4)));
typedef unsigned u32x8 __attribute__((ext_vector_type(8)));
typedef unsigned u32x16 __attribute__((ext_vector_type(16)));
typedef __bf16 bf16x2 __attribute__((ext_vector_type(2)));
typedef __bf16 bf16x8 __attribute__((ext_vector_type(8)));
typedef __bf16 bf16x16 __attribute__((ext_vector_type(16)));
typedef short s16x4 __attribute__((ext_vector_type(4)));
typedef unsigned long long u64;
#define LDSP(T, a) ((__attribute__((address_space(3))) T*)(uintptr_t)(a))

constexpr size_t WS_E = 0;
constexpr size_t WS_F1 = 1024;
constexpr size_t WS_F2 = 4096;
constexpr size_t WS_F3 = WS_F2 + 16384;
constexpr size_t WS_G = WS_F3 + 16384;
constexpr size_t WS_QKV = 65536;
constexpr size_t WS_H = WS_QKV + 32 * 196608;
constexpr size_t WS_O = WS_H + 86 * 65536;
constexpr size_t WS_JUNK = WS_O + 32 * 65536;
constexpr size_t WS_DBG = WS_JUNK + 2048 * 1024;
constexpr int LDS_BYTES = 163840;
constexpr int SLOT = 16384;
constexpr unsigned SPIN_CAP = 1u << 20;

#define MFMA16(a, b, c) __builtin_amdgcn_mfma_f32_16x16x32_bf16(__builtin_bit_cast(bf16x8, a), __builtin_bit_cast(bf16x8, b), c, 0, 0, 0)
#define MFMA32(a, b, c) __builtin_amdgcn_mfma_f32_32x32x16_bf16(__builtin_bit_cast(bf16x8, a), __builtin_bit_cast(bf16x8, b), c, 0, 0, 0)
#define WAITV_BAR(N) asm volatile("s_waitcnt vmcnt(" #N ") lgkmcnt(0)\n\ts_barrier" ::: "memory")
#define BAR() asm volatile("s_waitcnt lgkmcnt(0)\n\ts_barrier" ::: "memory")

__device__ __forceinline__ unsigned pk2(float lo, float hi) { f32x2 v = {lo, hi}; return __builtin_bit_cast(unsigned, __builtin_convertvector(v, bf16x2)); }
__device__ __forceinline__ float bflo(unsigned u) { return __uint_as_float(u << 16); }
__device__ __forceinline__ float bfhi(unsigned u) { return __uint_as_float(u & 0xffff0000u); }

__device__ __forceinline__ void glds2(unsigned voff0, unsigned voff1, const char* sbase, unsigned ldst) {
    unsigned sv;
    asm volatile("s_mov_b32 %0, m0\n\ts_mov_b32 m0, %4\n\ts_nop 0\n\tglobal_load_lds_dwordx4 %1, %3\n\t"
                 "s_mov_b32 m0, %5\n\ts_nop 0\n\tglobal_load_lds_dwordx4 %2, %3\n\ts_mov_b32 m0, %0"
                 : "=&s"(sv) : "v"(voff0), "v"(voff1), "s"(sbase), "s"(ldst), "s"(ldst + 1024u) : "memory");
}
__device__ __forceinline__ void glds3(unsigned voff0, unsigned voff1, unsigned voff2, const char* sbase, unsigned ldst) {
    unsigned sv;
    asm volatile("s_mov_b32 %0, m0\n\ts_mov_b32 m0, %5\n\ts_nop 0\n\tglobal_load_lds_dwordx4 %1, %4\n\t"
                 "s_mov_b32 m0, %6\n\ts_nop 0\n\tglobal_load_lds_dwordx4 %2, %4\n\t"
                 "s_mov_b32 m0, %7\n\ts_nop 0\n\tglobal_load_lds_dwordx4 %3, %4\n\ts_mov_b32 m0, %0"
                 : "=&s"(sv) : "v"(voff0), "v"(voff1), "v"(voff2), "s"(sbase), "s"(ldst), "s"(ldst + 1024u), "s"(ldst + 2048u) : "memory");
}
#define KV_SC " sc1"
__device__ __forceinline__ void glds1_sc1(unsigned voff, const char* sbase, unsigned ldst) {
    unsigned sv;
    asm volatile("s_mov_b32 %0, m0\n\ts_mov_b32 m0, %3\n\ts_nop 0\n\tglobal_load_lds_dwordx4 %1, %2" KV_SC "\n\ts_mov_b32 m0, %0"
                 : "=&s"(sv) : "v"(voff), "s"(sbase), "s"(ldst) : "memory");
}
__device__ __forceinline__ void st16_sc1(const char* sbase, unsigned voff, u32x4 v) {
    asm volatile("global_store_dwordx4 %0, %1, %2 sc1\n\ts_nop 1" :: "v"(voff), "v"(v), "s"(sbase) : "memory");
}
__device__ __forceinline__ void st8_sc1(const char* sbase, unsigned voff, u64 v) {
    asm volatile("global_store_dwordx2 %0, %1, %2 sc1" :: "v"(voff), "v"(v), "s"(sbase) : "memory");
}
__device__ __forceinline__ void st4_sc1(const char* sbase, unsigned voff, unsigned v) {
    asm volatile("global_store_dword %0, %1, %2 sc1" :: "v"(voff), "v"(v), "s"(sbase) : "memory");
}
__device__ __forceinline__ u64 ld8_sc1_wait(const char* sbase, unsigned voff) {
    u64 v;
    asm volatile("global_load_dwordx2 %0, %1, %2 sc1\n\ts_waitcnt vmcnt(0)" : "=&v"(v) : "v"(voff), "s"(sbase) : "memory");
    return v;
}
__device__ __forceinline__ void poll_flags(const char* sbase, unsigned voff, u64 tag) {
    for (unsigned spins = 0; spins < SPIN_CAP; ++spins) {
        const u64 v = ld8_sc1_wait(sbase, voff);
        if (__all(v == tag)) break;
        __builtin_amdgcn_s_sleep(2);
    }
}
__device__ __forceinline__ u32x2 tr_read(unsigned a) {
    return __builtin_bit_cast(u32x2, __builtin_amdgcn_ds_read_tr16_b64_v4i16(LDSP(s16x4, a)));
}
__device__ __forceinline__ u32x4 tr_frag(unsigned a, unsigned second) {
    const u32x2 lo = tr_read(a), hi = tr_read(a + second);
    return (u32x4){lo[0], lo[1], hi[0], hi[1]};
}
__device__ __forceinline__ u32x4 pack_block(const f32x4& t0, const f32x4& t1) {
    unsigned a0 = pk2(t0[0], t0[1]), a1 = pk2(t0[2], t0[3]), b0 = pk2(t1[0], t1[1]), b1 = pk2(t1[2], t1[3]);
    auto r0 = __builtin_amdgcn_permlane16_swap(a0, b0, false, false);
    auto r1 = __builtin_amdgcn_permlane16_swap(a1, b1, false, false);
    return (u32x4){r0[0], r1[0], r0[1], r1[1]};
}
__device__ __forceinline__ u32x4 pack8(float a, float b, float c, float d, float e, float f, float g, float h) {
    auto r0 = __builtin_amdgcn_permlane32_swap(pk2(a, b), pk2(e, f), false, false);
    auto r1 = __builtin_amdgcn_permlane32_swap(pk2(c, d), pk2(g, h), false, false);
    return (u32x4){r0[0], r1[0], r0[1], r1[1]};
}
__device__ __forceinline__ float rowmax16(const f32x16& s) {
    float m = fmaxf(fmaxf(s[0], s[1]), s[2]);
#pragma unroll
    for (int r = 3; r < 15; r += 2) m = fmaxf(fmaxf(m, s[r]), s[r + 1]);
    m = fmaxf(m, s[15]);
    auto rr = __builtin_amdgcn_permlane32_swap(__float_as_uint(m), __float_as_uint(m), false, false);
    return fmaxf(__uint_as_float(rr[0]), __uint_as_float(rr[1]));
}
template <int CTRL> __device__ __forceinline__ float dpp_add(float v) {
    return v + __uint_as_float(__builtin_amdgcn_update_dpp(0u, __float_as_uint(v), CTRL, 0xf, 0xf, true));
}

#define RSTAMP(i) do { } while (0)
#define STAMP(i) do { } while (0)

}

__global__ void __launch_bounds__(512, 2)
gated_mix_fused_kernel(const char* __restrict__ x_, const char* __restrict__ wqkv_, const char* __restrict__ wo_, const char* __restrict__ wg_,
                       const char* __restrict__ wu_, const char* __restrict__ wd_, const char* __restrict__ gate_w_, char* __restrict__ out_, char* ws_) {
    u32x16 ka; u64 kws;
    asm volatile("s_load_dwordx16 %0, %2, 0x0\n\ts_load_dwordx2 %1, %2, 0x40" : "=&s"(ka), "=&s"(kws) : "s"(__builtin_amdgcn_kernarg_segment_ptr()));
    constexpr unsigned L0 = 0;
    const int lane = threadIdx.x & 63;
    const int w = __builtin_amdgcn_readfirstlane(threadIdx.x >> 6);
    const int bid = blockIdx.x, xcd = bid & 7, jb = bid >> 3;
    STAMP(0);

    const int tb = 8 * (xcd & 1) + (jb & 7), cg = 4 * (xcd >> 1) + (jb >> 3);
    const int tb32 = w & 3, hf = w >> 2;
    const int u0 = (cg * 80) / 15;
#define KPTR(i) ((char*)(((u64)ka[2 * (i) + 1] << 32) | ka[2 * (i)]))
    unsigned vx[2], vq[2], vf[3];
#pragma unroll
    for (int par = 0; par < 2; ++par) {
        const int row = 16 * w + 8 * par + (lane >> 3);
        const int c16 = (lane & 7) ^ ((4 * par + (lane >> 4)) & 7);
        vx[par] = (unsigned)((128 * tb + row) * 1024 + c16 * 16);
    }
#pragma unroll
    for (int i = 0; i < 3; ++i) {
        const int q = 64 * i + lane, rl = q / 12, p = q - 12 * rl;
        const int b = (rl >> 3) & 1;
        vf[i] = (unsigned)((16 * (w & 3) + rl) * 2752 + 32 * u0 + 32 * ((p >> 1) ^ b ^ hf) + 16 * (p & 1));
    }
    const int wq_piece0 = w < 4 ? w : 4 + 2 * (w - 4);
#pragma unroll
    for (int i = 0; i < 2; ++i) {
        const int q = 64 * (wq_piece0 + i) + lane, row = q / 12, p = q - 12 * row;
        vq[i] = (unsigned)(row * 3072 + 192 * cg + 32 * ((p >> 1) ^ ((row >> 3) & 1)) + 16 * (p & 1));
    }
#define ROT 1
    unsigned fidx = ROT ? (unsigned)((tb + 2 * cg) & 7) : 0u;
    __builtin_amdgcn_sched_barrier(0);
    asm volatile("s_waitcnt lgkmcnt(0)" : "+s"(ka), "+s"(kws));
    const char* const x = KPTR(0); const char* const wqkv = KPTR(1); const char* const wo = KPTR(2); const char* const wg = KPTR(3);
    const char* const wu = KPTR(4); const char* const wd = KPTR(5); const char* const gate_w = KPTR(6); char* const out = KPTR(7); char* const ws = (char*)kws;
    const char* fp_ = (w < 4) ? wg : wu;
#define PF_W 0
    unsigned pfo;
    {
        const int l48 = lane < 48 ? lane : 47, r = l48 / 3, c = l48 - 3 * r;
        const int col = 32 * u0 + 128 * c;
        pfo = PF_W ? (unsigned)((64 * (tb & 7) + 16 * (w & 3) + r) * 2752 + (col < 2748 ? col : 2748)) : 0u;
    }
    const int l15 = lane & 15, l31 = lane & 31, hh = lane >> 5, gsel = (lane >> 4) & 1, kq4 = lane >> 4;
    const unsigned sxo = (unsigned)(w * 2048), swo = (unsigned)(w * 3072);
    const unsigned xa = (unsigned)((32 * tb32 + l31) * 128 + ((hh ^ ((l31 >> 1) & 7)) * 16));
    const unsigned wrow = (unsigned)((8 * hh + (l15 >> 2)) * 192 + 8 * (lane & 3));
    u32x16 X0, X1, X2, X3, X4, X5, X6, X7;
    f32x16 A0, A1;
    u64 epoch;
    unsigned m0s;
#define P1_OUT "=&{v[0:15]}"(A0), "=&{v[128:143]}"(X0), "=&{v[144:159]}"(X1), "=&{v[160:175]}"(X2), "=&{v[176:191]}"(X3), "=&{v[192:207]}"(X4), \
               "=&{v[208:223]}"(X5), "=&{v[224:239]}"(X6), "=&{v[240:255]}"(X7), [m0s] "=&s"(m0s), [ep] "=&v"(epoch), [fi] "+s"(fidx)
#define P1_IN [vx0] "v"(vx[0]), [vx1] "v"(vx[1]), [vq0] "v"(vq[0]), [vf0] "v"(vf[0]), [vf1] "v"(vf[1]), [vf2] "v"(vf[2]), \
              [xa] "v"(xa), [sx] "s"(sxo), [sw] "s"(swo), [sq] "s"((unsigned)(wq_piece0 * 1024)), [epp] "s"(ws + WS_E), \
              "{s[40:41]}"(x), "{s[42:43]}"(wqkv), "{s[46:47]}"(fp_) P1_PF
#define P1_ST
#define P1_PF , [pfo] "v"(pfo)
#define P1_CLOB "memory", "scc", "s44", "s45", "s52", "s53", "v48", "v49", "v50", "v51", "v52", "v53", "v54", "v55", "v56", "v57", "v58", "v59", "v60", "v61", "v62", "v63", "v64", "v65", "v66", "v67", \
                "v68", "v69", "v70", "v71", "v72", "v73", "v74", "v75", "v76", "v77", "v78", "v79", "v80", "v81", "v82", "v83", "v84"
    if (hf == 0) {
        const unsigned wq0 = 65536u + wrow + 32 * ((0 + gsel) ^ hh), wq1 = 65536u + wrow + 32 * ((2 + gsel) ^ hh);
        asm volatile(P1_ASM_2 : P1_OUT, "=&{v[16:31]}"(A1) P1_ST : P1_IN, [wq0] "v"(wq0), [wq1] "v"(wq1) : P1_CLOB);
    } else {
        const unsigned wq0 = 65536u + wrow + 32 * ((4 + gsel) ^ hh);
        asm volatile(P1_ASM_1 : P1_OUT P1_ST : P1_IN, [wq0] "v"(wq0), [vq1] "v"(vq[1]) : P1_CLOB);
        A1 = (f32x16){};
    }
    const u64 tag = epoch + 1;
    STAMP(2);
    {
        const int tile32 = 4 * tb + tb32;
        const unsigned scr = L0 + 139264 + w * 1024;
        const char* qkv_t = ws + WS_QKV + (size_t)tile32 * 3072;
#pragma unroll
        for (int i = 0; i < 4; ++i) {
            if (i >= 2 && hf) break;
            const f32x16& A = i < 2 ? A0 : A1;
            const int T = 6 * cg + 4 * hf + i, ty = T >> 5, head = T & 31, r0 = 8 * (i & 1);
            const float sc = (ty == 0) ? 0.25f * 1.4426950408889634f : 1.0f;
            const unsigned voff = (unsigned)(head * 196608 + lane * 16);
            const u32x4 v = pack8(A[r0] * sc, A[r0 + 1] * sc, A[r0 + 2] * sc, A[r0 + 3] * sc, A[r0 + 4] * sc, A[r0 + 5] * sc, A[r0 + 6] * sc, A[r0 + 7] * sc);
            if (ty == 2) {
                *LDSP(u32x4, scr + l31 * 32 + hh * 16) = v;
                const u32x4 vt = tr_frag(scr + (8 * kq4 + (l15 >> 2)) * 32 + 8 * (lane & 3), 128);
                st16_sc1(qkv_t, voff + 1024u, vt);
            } else {
                st16_sc1(qkv_t, voff + (ty == 0 ? 2048u : 0u), v);
            }
        }
    }
    STAMP(3);
    const int ah = 4 * xcd + (jb >> 3), aqb = jb & 7;
    unsigned f1off;
    {
        const int cgq = ah / 6, cgk = (32 + ah) / 6, cgv = (64 + ah) / 6;
        const int li = lane < 34 ? lane : 33;
        const int fcg = li < 16 ? cgk : (li < 32 ? cgv : cgq), ftb = li < 32 ? (li & 15) : 2 * aqb + (li - 32);
        f1off = (unsigned)((fcg * 16 + ftb) * 8);
    }
    f32x16 G0, G1, G2;
    constexpr unsigned KVB = 24576;
    const char* kvh = ws + WS_QKV + (size_t)ah * 196608 + (w & 1) * 1024;
    unsigned kvok;
    const unsigned kvt0 = (unsigned)((ROT ? 8 * aqb : 0) + (w >> 1));
    {
        const unsigned fb_ = wrow + gsel * 12288;
        const unsigned l0 = fb_ + 32 * ((3 * hf + 0) ^ hh ^ gsel), l1 = fb_ + 32 * ((3 * hf + 1) ^ hh ^ gsel), l2 = fb_ + 32 * ((3 * hf + 2) ^ hh ^ gsel);
        asm volatile(P2_ASM
            : "=&{v[0:15]}"(G0), "=&{v[16:31]}"(G1), "=&{v[32:47]}"(G2), [m0s] "=&s"(m0s), [fi] "+s"(fidx), [kvok] "=&s"(kvok)
            : "{s[46:47]}"(fp_), "{s[48:49]}"(kvh), "{v[128:143]}"(X0), "{v[144:159]}"(X1), "{v[160:175]}"(X2), "{v[176:191]}"(X3), "{v[192:207]}"(X4), "{v[208:223]}"(X5), "{v[224:239]}"(X6), "{v[240:255]}"(X7),
              [vf0] "v"(vf[0]), [vf1] "v"(vf[1]), [vf2] "v"(vf[2]), [wfl0] "v"(l0), [wfl1] "v"(l1), [wfl2] "v"(l2),
              [wfh0] "v"(l0 + 65536u), [wfh1] "v"(l1 + 65536u), [wfh2] "v"(l2 + 65536u), [sw] "s"(swo), [sx] "s"(sxo),
              [vz] "v"(0u), [tag] "v"(tag), [f1p] "s"(ws + WS_F1 + (size_t)(cg * 16 + tb) * 8), [f1o] "v"(f1off), [f1b] "s"(ws + WS_F1),
              [t0] "s"(kvt0), [skv] "s"(KVB + (unsigned)(w * 1024)), [vl16] "v"((unsigned)(lane * 16))
            : "memory", "scc", "vcc", "s44", "s45", "v48", "v49", "v50", "v51", "v52", "v53", "v54", "v55", "v56", "v57", "v58", "v59", "v60", "v61", "v62", "v63", "v64", "v65", "v66", "v67", "v68", "v69", "v70", "v71", "v72", "v73");
    }
    STAMP(6); RSTAMP(0);
    int tz_ = threadIdx.x; asm volatile("" : "+v"(tz_));
    {
    const int lane = tz_ & 63, l15 = lane & 15, l31 = lane & 31, hh = lane >> 5, kq4 = lane >> 4;
    (void)l31; (void)hh; (void)kq4;
    const int qg = w & 3, kh = w >> 2;
    BAR();
    STAMP(7);
    u32x4 qa, qbf, gx, gw;
    const int gtok = 128 * tb + 8 * cg + w;
    const char* qp = ws + WS_QKV + (size_t)ah * 196608 + (size_t)(8 * aqb + 2 * qg) * 3072 + 2048;
#define Q_LOAD() asm volatile("global_load_dwordx4 %0, %2, %3 sc1\n\tglobal_load_dwordx4 %1, %2, %3 offset:3072 sc1" : "=&{v[96:99]}"(qa), "=&{v[100:103]}"(qbf) : "v"((unsigned)(lane * 16)), "s"(qp) : "memory")
#define KV_DMA(i) glds1_sc1((unsigned)(lane * 16), kvh + ((kvt0 + 4 * (i)) & 63) * 3072, KVB + ((i) * 8 + w) * 1024)
    asm volatile("global_load_dwordx4 %0, %2, %3\n\tglobal_load_dwordx4 %1, %2, %4" : "=&v"(gx), "=&v"(gw)
                 : "v"((unsigned)(lane * 16)), "s"(x + (size_t)gtok * 1024), "s"(gate_w) : "memory");
    if (kvok) { Q_LOAD(); KV_DMA(5); KV_DMA(6); KV_DMA(7); }
    {
        const int tile32 = 4 * tb + tb32;
#pragma unroll
        for (int i = 0; i < 3; ++i) {
            const f32x16& G = i == 0 ? G0 : i == 1 ? G1 : G2;
            float hv[8];
#pragma unroll
            for (int r = 0; r < 8; r += 2) {
                const f32x2 g = {G[r], G[r + 1]}, u = {G[r + 8], G[r + 9]};
                const f32x2 t = g * __builtin_elementwise_fma(g * g, (f32x2){-0.10294324f, -0.10294324f}, (f32x2){-2.30220820f, -2.30220820f});
                const f32x2 d = (f32x2){__builtin_amdgcn_exp2f(t[0]), __builtin_amdgcn_exp2f(t[1])} + 1.0f;
                const f32x2 hh2 = (g * u) * (f32x2){__builtin_amdgcn_rcpf(d[0]), __builtin_amdgcn_rcpf(d[1])};
                hv[r] = hh2[0]; hv[r + 1] = hh2[1];
            }
            const u32x4 v = pack8(hv[0], hv[1], hv[2], hv[3], hv[4], hv[5], hv[6], hv[7]);
            const bool dup = i == 0 && hf == 0 && cg > 0 && u0 < ((cg - 1) * 80) / 15 + 6;
            st16_sc1(dup ? ws + WS_JUNK + (size_t)(bid * 8 + w) * 1024 : ws + WS_H + (size_t)(u0 + 3 * hf + i) * 65536 + (size_t)tile32 * 1024, (unsigned)(lane * 16), v);
        }
        asm volatile("s_waitcnt vmcnt(3)" : "+v"(gx), "+v"(gw) :: "memory");
        float acc = 0.f;
#pragma unroll
        for (int e = 0; e < 4; ++e) { acc = __builtin_fmaf(bflo(gx[e]), bflo(gw[e]), acc); acc = __builtin_fmaf(bfhi(gx[e]), bfhi(gw[e]), acc); }
        acc = dpp_add<0x111>(acc); acc = dpp_add<0x112>(acc); acc = dpp_add<0x114>(acc); acc = dpp_add<0x118>(acc);
        const float tot = __uint_as_float(__builtin_amdgcn_readlane(__float_as_uint(acc), 15)) + __uint_as_float(__builtin_amdgcn_readlane(__float_as_uint(acc), 31))
                        + __uint_as_float(__builtin_amdgcn_readlane(__float_as_uint(acc), 47)) + __uint_as_float(__builtin_amdgcn_readlane(__float_as_uint(acc), 63));
        const float gate1 = __builtin_amdgcn_rcpf(1.0f + __builtin_amdgcn_exp2f(-1.4426950408889634f * tot));
        if (lane == 0) st4_sc1(ws + WS_G + (size_t)gtok * 4, 0u, __float_as_uint(gate1));
    }
    STAMP(8);
    if (!kvok) {
        poll_flags(ws + WS_F1, f1off, tag);
        Q_LOAD();
#pragma unroll
        for (int i = 0; i < 16; ++i) KV_DMA(i);
        asm volatile("s_waitcnt vmcnt(0)" ::: "memory");
    }
    asm volatile("s_barrier" : "+{v[96:99]}"(qa), "+{v[100:103]}"(qbf) :: "memory");
    STAMP(9); RSTAMP(1);

    const int tb64 = 4 * xcd + (jb & 3), cb = jb >> 2;
    const int ptt = w & 1, pkq = w >> 1;
    const int fb0 = pkq * 22 - (pkq == 3 ? 1 : 0);
    const int nfb = pkq < 2 ? 22 : 21;

    const unsigned kaddr0 = L0 + KVB + lane * 16;
    const f32x16 z16 = {};
    f32x16 OA = {}, OB = {}, SA, SB = {};
    f32x16 NMA, NMB;
    {
        const u32x4 k0 = *LDSP(const u32x4, kaddr0);
        const float ma = rowmax16(MFMA32(k0, qa, z16)), mb = rowmax16(MFMA32(k0, qbf, z16));
#pragma unroll
        for (int r = 0; r < 16; ++r) { NMA[r] = -ma; NMB[r] = -mb; }
    }
#define SPARSE_ONES 1
    const unsigned one2 = (!SPARSE_ONES || (l31 & 27) == 16) ? 0x3F803F80u : 0u;
    const u32x4 ones = {one2, one2, one2, one2};
    u32x4 K0, K1, V00 = ones, V01 = ones, V10 = ones, V11 = ones;
    const unsigned idx = 0xDDDDDDDDu;
    unsigned kaddr = kaddr0 + kh * 65536, vaddr = L0 + KVB + 1024 + (lane >> 5) * 256 + l15 * 16 + kh * 65536;
    const u64 msk = 0x0000FFFF0000FFFFull;
    asm volatile(ATT_PRO_ASM
        : "={v[32:47]}"(SA), "={v[104:107]}"(K0), "={v[108:111]}"(K1), "+{v[112:115]}"(V00), "+{v[120:123]}"(V10)
        : "{v[64:79]}"(NMA), "{v[96:99]}"(qa), "{v129}"(kaddr), "{v130}"(vaddr), [msk] "s"(msk) : "memory");
#define PUBLISH_F2() do { asm volatile("s_waitcnt vmcnt(0)" ::: "memory"); if (lane == 0) st8_sc1(ws + WS_F2 + (size_t)((tb * 16 + cg) * 8 + w) * 8, 0u, tag); } while (0)
    if (kh) PUBLISH_F2();
    STAMP(10);
#define ATT_STATE "+{v[0:15]}"(OA), "+{v[16:31]}"(OB), "+{v[32:47]}"(SA), "+{v[48:63]}"(SB), "+{v[104:107]}"(K0), "+{v[108:111]}"(K1), \
                  "+{v[112:115]}"(V00), "+{v[116:119]}"(V01), "+{v[120:123]}"(V10), "+{v[124:127]}"(V11), "+{v129}"(kaddr), "+{v130}"(vaddr), [cnt] "+s"(cnt)
#define ATT_CONST "{v[64:79]}"(NMA), "{v[80:95]}"(NMB), "{v[96:99]}"(qa), "{v[100:103]}"(qbf), "{v128}"(idx), [msk] "s"(msk)
    int cnt = 1;
    asm volatile(ATT_LOOP_BODY : ATT_STATE : ATT_CONST : "memory", "scc");
    if (!kh) PUBLISH_F2();
    cnt = 1;
    asm volatile(ATT_LOOP_BODY : ATT_STATE : ATT_CONST : "memory", "scc");

    const bool epoch_wave = bid == 0 && w == 1;
    u32x8 f1all;
#define EARLY_EPOCH 1
    if (EARLY_EPOCH && epoch_wave) asm volatile("global_load_dwordx4 v[220:223], %1, %2 sc1\n\tglobal_load_dwordx4 v[224:227], %1, %2 offset:16 sc1" : "=&{v[220:227]}"(f1all) : "v"((unsigned)(lane * 32)), "s"(ws + WS_F1) : "memory");
    u32x4 f2v;
    const unsigned f2off = (unsigned)((tb64 >> 1) * 1024 + lane * 16);
    cnt = 2;
#define PF_D 1
    const char* pfd;
    {
        const int ln = (jb * 8 + w) * 59 + (lane < 59 ? lane : 58);
        pfd = !PF_D ? wo : ln < 11008 ? wd + (size_t)ln * 128 : wo + (size_t)(ln - 11008) * 128;
    }
    asm volatile("global_load_dwordx4 %[fv], %[fo], %[fb] sc1\n\tglobal_load_dword v131, %[pfd], off\n\t" ATT_LOOP_BODY "s_waitcnt vmcnt(0)"
        : ATT_STATE, [fv] "=&v"(f2v) : ATT_CONST, [fo] "v"(f2off), [fb] "s"(ws + WS_F2), [pfd] "v"(pfd) : "memory", "scc", "v131");
    STAMP(11);
    bool epoch_done = false;
    if (EARLY_EPOCH && epoch_wave) {
        asm volatile("" : "+{v[220:227]}"(f1all));
        const unsigned tl = (unsigned)tag, th = (unsigned)(tag >> 32);
        epoch_done = __all(f1all[0] == tl && f1all[1] == th && f1all[2] == tl && f1all[3] == th && f1all[4] == tl && f1all[5] == th && f1all[6] == tl && f1all[7] == th);
        if (epoch_done && lane == 0) st8_sc1(ws + WS_E, 0u, tag);
    }
    if (!__all(f2v[0] == (unsigned)tag && f2v[1] == (unsigned)(tag >> 32) && f2v[2] == (unsigned)tag && f2v[3] == (unsigned)(tag >> 32))) {
        poll_flags(ws + WS_F2, f2off, tag); poll_flags(ws + WS_F2, f2off + 8u, tag);
    }
    STAMP(12);
    u32x16 H0, H1, H2, H3, H4; u32x8 H5; float gate;
    {
        const char* hp = ws + WS_H + (size_t)fb0 * 65536 + (size_t)(2 * tb64 + ptt) * 1024;
        const unsigned linc = pkq < 2 ? 0x10000u : 0u;
        cnt = 4;
        asm volatile("global_load_dword %[g], %[go], %[gb] sc1\n\t" ATT_HLOAD_ASM ATT_LOOP_BODY "s_waitcnt vmcnt(0)"
            : ATT_STATE, "=&{v[132:147]}"(H0), "=&{v[148:163]}"(H1), "=&{v[164:179]}"(H2), "=&{v[180:195]}"(H3), "=&{v[196:211]}"(H4), "=&{v[212:219]}"(H5),
              "+{s[40:41]}"(hp), [g] "=&v"(gate)
            : ATT_CONST, [hvo] "v"((unsigned)(lane * 16)), [linc] "s"(linc), [go] "v"((unsigned)((lane & 31) * 4)), [gb] "s"(ws + WS_G + (size_t)(64 * tb64 + 32 * ptt) * 4)
            : "memory", "scc");
    }
    asm volatile(ATT_EPI_ASM : "+{v[16:31]}"(OB) : "{v[48:63]}"(SB), "{v[124:127]}"(V11), "{v128}"(idx) : "memory");
    STAMP(13); RSTAMP(2);
    {
        const unsigned xb = L0 + w * 3072;
        const f32x16& snd = kh ? OA : OB;
        *LDSP(f32x4, xb + lane * 16) = (f32x4){snd[0], snd[1], snd[2], snd[3]};
        *LDSP(f32x4, xb + 1024 + lane * 16) = (f32x4){snd[4], snd[5], snd[6], snd[7]};
        *LDSP(float, xb + 2048 + lane * 4) = snd[8];
    }
    BAR();
    {
        float o[8], l;
        const unsigned pb = L0 + (w ^ 4) * 3072;
        const f32x4 s0 = *LDSP(const f32x4, pb + lane * 16), s1 = *LDSP(const f32x4, pb + 1024 + lane * 16);
        const float sl = *LDSP(const float, pb + 2048 + lane * 4);
#pragma unroll
        for (int r = 0; r < 4; ++r) { o[r] = (kh ? OB[r] : OA[r]) + s0[r]; o[r + 4] = (kh ? OB[r + 4] : OA[r + 4]) + s1[r]; }
        l = (kh ? OB[8] : OA[8]) + sl;
        bool bad = !(l < 1.0e18f);
#pragma unroll
        for (int r = 0; r < 8; ++r) bad |= !(__builtin_fabsf(o[r]) < 3.0e38f);
        if (__builtin_expect(__any(bad), 0)) {
            const u32x4 qf = kh ? qbf : qa;
            float mr = -INFINITY;
            f32x16 acc = {};
#pragma unroll 1
            for (int t = 0; t < 64; ++t) {
                const u32x4 kf = *LDSP(const u32x4, kaddr0 + t * 2048);
                u32x4 v0 = ones, v1 = ones;
                if (!(lane & 16)) {
                    const unsigned va = L0 + KVB + 1024 + (lane >> 5) * 256 + l15 * 16 + t * 2048;
                    v0 = *LDSP(const u32x4, va); v1 = *LDSP(const u32x4, va + 512);
                }
                const f32x16 s = MFMA32(kf, qf, z16);
                const float mn = fmaxf(mr, rowmax16(s));
                const float al = __builtin_amdgcn_exp2f(mr - mn);
                mr = mn;
                f32x8 pa, pb2;
#pragma unroll
                for (int r = 0; r < 8; ++r) { pa[r] = __builtin_amdgcn_exp2f(s[r] - mn); pb2[r] = __builtin_amdgcn_exp2f(s[r + 8] - mn); }
                acc *= al;
                acc = __builtin_amdgcn_smfmac_f32_32x32x32_bf16(__builtin_bit_cast(bf16x8, v0), __builtin_bit_cast(bf16x16, pa), acc, (int)idx, 0, 0);
                acc = __builtin_amdgcn_smfmac_f32_32x32x32_bf16(__builtin_bit_cast(bf16x8, v1), __builtin_bit_cast(bf16x16, pb2), acc, (int)idx, 0, 0);
            }
#pragma unroll
            for (int r = 0; r < 8; ++r) o[r] = acc[r];
            l = acc[8];
        }
        const float rl = __builtin_amdgcn_rcpf(l);
        const unsigned a0 = pk2(o[0] * rl, o[1] * rl), a1 = pk2(o[2] * rl, o[3] * rl), b0 = pk2(o[4] * rl, o[5] * rl), b1 = pk2(o[6] * rl, o[7] * rl);
        auto r0 = __builtin_amdgcn_permlane32_swap(a0, b0, false, false);
        auto r1 = __builtin_amdgcn_permlane32_swap(a1, b1, false, false);
        st16_sc1(ws + WS_O + (size_t)ah * 65536 + (size_t)(8 * aqb + 2 * qg + kh) * 1024, (unsigned)(lane * 16), (u32x4){r0[0], r1[0], r0[1], r1[1]});
    }
    STAMP(14);
    BAR();
    STAMP(15); RSTAMP(3);
    unsigned vw[2];
#pragma unroll
    for (int par = 0; par < 2; ++par) { const int row = 8 * par + (lane >> 3); vw[par] = (unsigned)(row * 1024 + 128 * cb + 16 * ((lane & 7) ^ (4 * ((row >> 1) & 1)))); }
    const int fkq = w >> 1, fst = w & 1;
    const int ffb0 = fkq * 22 - (fkq == 3 ? 1 : 0);
    unsigned pa0_, pa1_;
    {
        const int g = lane >> 4, q = l15 >> 2, pp = lane & 3, row = 8 * (g >> 1) + q, c = 2 * (g & 1) + (pp >> 1);
        const unsigned a = (unsigned)(row * 128 + ((c ^ (4 * (q >> 1))) * 16) + 8 * (pp & 1));
        pa0_ = a + pkq * 4096; pa1_ = (a ^ 64u) + pkq * 4096;
    }
    f32x16 ab0, ab1, aa0, aa1;
    {
        const char* wdp = wd + (size_t)(ffb0 + fst) * 16384;
        const char* wop = wo + (size_t)(8 * fkq + fst) * 16384;
        const char* op = ws + WS_O + (size_t)(8 * pkq) * 65536 + (size_t)(2 * tb64 + ptt) * 1024;
        const unsigned lb = (fkq >= 2 && fst == 1) ? 16384u : 32768u;
        const unsigned f3off = (unsigned)(((2 * tb64 + ptt) * 32 + 8 * pkq + (lane & 7)) * 8);
        unsigned m0s2;
        const f32x2 g1p = {1.0f - gate, 1.0f - gate};
        asm volatile(P4_ASM
            : "=&{v[0:15]}"(ab0), "=&{v[16:31]}"(ab1), "=&{v[64:79]}"(aa0), "=&{v[80:95]}"(aa1), [m0s] "=&s"(m0s2),
              "+{s[40:41]}"(wdp), "+{s[42:43]}"(wop), "+{s[44:45]}"(op)
            : "{v[132:147]}"(H0), "{v[148:163]}"(H1), "{v[164:179]}"(H2), "{v[180:195]}"(H3), "{v[196:211]}"(H4), "{v[212:219]}"(H5),
              [pa0] "v"(pa0_), [pa1] "v"(pa1_), [vw0] "v"(vw[0]), [vw1] "v"(vw[1]), [sw] "s"((unsigned)(w * 2048)), [lb] "s"(lb),
              [vo] "v"((unsigned)(lane * 16)), [f3o] "v"(f3off), [f3b] "s"(ws + WS_F3), [tag] "v"(tag),
              [fmy] "s"(ws + WS_F3 + (size_t)((8 * aqb + 2 * qg + kh) * 32 + ah) * 8), [vz] "v"(0u), [pkq] "s"(pkq), [g1] "v"(g1p)
            : "memory", "scc", "vcc", "s46",
              "v32", "v33", "v34", "v35", "v36", "v37", "v38", "v39", "v40", "v41", "v42", "v43", "v44", "v45", "v46", "v47",
              "v48", "v49", "v50", "v51", "v52", "v53", "v54", "v55", "v56", "v57", "v58", "v59", "v60", "v61", "v62", "v63",
              "v96", "v97", "v98", "v99", "v100", "v101", "v102", "v103", "v104", "v105", "v106", "v107", "v108", "v109", "v110", "v111",
              "v112", "v113", "v114", "v115", "v116", "v117", "v118", "v119", "v120", "v121", "v122", "v123");
    }
    STAMP(21); RSTAMP(4);
    {
        const int tok = lane & 31, hh2 = lane >> 5;
        const f32x2 gp = {gate, gate};
#pragma unroll
        for (int nt = 0; nt < 2; ++nt) {
            unsigned pk[8];
#pragma unroll
            for (int r = 0; r < 16; r += 2) {
                const f32x2 av = {(nt ? aa1 : aa0)[r], (nt ? aa1 : aa0)[r + 1]}, bv = {(nt ? ab1 : ab0)[r], (nt ? ab1 : ab0)[r + 1]};
                const f32x2 v = __builtin_elementwise_fma(gp, av, bv);
                pk[r >> 1] = pk2(v[0], v[1]);
            }
#pragma unroll
            for (int p = 0; p < 2; ++p) {
                auto r0 = __builtin_amdgcn_permlane32_swap(pk[4 * p], pk[4 * p + 2], false, false);
                auto r1 = __builtin_amdgcn_permlane32_swap(pk[4 * p + 1], pk[4 * p + 3], false, false);
                const int ch = 4 * nt + 2 * p + hh2;
                *LDSP(u32x4, L0 + w * 4096 + tok * 128 + ((ch ^ (tok & 7)) * 16)) = (u32x4){r0[0], r1[0], r0[1], r1[1]};
            }
        }
    }
    BAR();
    {
        const int tok = 8 * pkq + (lane >> 3), c8 = lane & 7;
        float s[8] = {};
#pragma unroll
        for (int k = 0; k < 4; ++k) {
            const u32x4 p = *LDSP(const u32x4, L0 + (unsigned)((2 * k + ptt) * 4096 + tok * 128 + ((c8 ^ (tok & 7)) * 16)));
#pragma unroll
            for (int e = 0; e < 4; ++e) { s[2 * e] += bflo(p[e]); s[2 * e + 1] += bfhi(p[e]); }
        }
        const u32x4 v = {pk2(s[0], s[1]), pk2(s[2], s[3]), pk2(s[4], s[5]), pk2(s[6], s[7])};
        st16_sc1(out + (size_t)(64 * tb64 + 32 * ptt) * 1024 + 128 * cb, (unsigned)(tok * 1024 + 16 * c8), v);
    }
    STAMP(22); RSTAMP(5);
    if (epoch_wave && !epoch_done) {
#pragma unroll 1
        for (int k = 0; k < 4; ++k) poll_flags(ws + WS_F1, (unsigned)((k * 64 + lane) * 8), tag);
        if (lane == 0) st8_sc1(ws + WS_E, 0u, tag);
    }
    }
}

extern "C" void kernel_launch(void* const* d_in, const int* in_sizes, int n_in, void* d_out, int out_size, void* d_ws, size_t ws_size,
                              hipStream_t stream) {
    (void)in_sizes; (void)n_in; (void)out_size; (void)ws_size;
    (void)hipFuncSetAttribute((const void*)gated_mix_fused_kernel, hipFuncAttributeMaxDynamicSharedMemorySize, LDS_BYTES);
    gated_mix_fused_kernel<<<256, 512, LDS_BYTES, stream>>>((const char*)d_in[0], (const char*)d_in[1], (const char*)d_in[2], (const char*)d_in[4],
                                                            (const char*)d_in[5], (const char*)d_in[6], (const char*)d_in[7], (char*)d_out, (char*)d_ws);
}
```
